```python
import torch

def _softmax(x, axis=-1):
    return torch.softmax(x, dim=axis)
def _gelu(x):
    return torch.nn.functional.gelu(x, approximate="tanh")
def _silu(x):
    return torch.nn.functional.silu(x)
def _sigmoid(x):
    return torch.sigmoid(x)
def _layernorm(x, axis=-1, eps=1e-5):
    m = x.mean(dim=axis, keepdim=True)
    v = x.var(dim=axis, keepdim=True, unbiased=False)
    return (x - m) / torch.sqrt(v + eps)
def _scatter_add(tgt, idx, src, axis=0):
    return tgt.clone().index_add_(axis, idx, src)
def _segment_sum(src, seg, n):
    out = torch.zeros((n,) + src.shape[1:], dtype=src.dtype)
    return out.index_add_(0, seg, src)
def _pad(x, pw):
    flat = tuple(v for pair in reversed(pw) for v in pair)
    return torch.nn.functional.pad(x, flat)

kernel_name = 'concat_heads__mha_block_cached__sliding_block_cached__decode'

def setup_inputs():

    _w = (lambda a: (lambda t: t.to(torch.bfloat16) if t.is_floating_point() else t)(torch.as_tensor(a)))
    def _A_setup():
        B, Sq, D = 4, 1, 1024
        H, Sk, Dh = 32, 8192, 128
        return {
            "x": _w((lambda *s: torch.randn(*s, dtype=torch.bfloat16) * 0.8)(B, Sq, D)),
            "k_cache": _w((lambda *s: torch.randn(*s, dtype=torch.bfloat16) * 1.6)(B, H, Sk, Dh)),
            "v_cache": _w((lambda *s: torch.randn(*s, dtype=torch.bfloat16) * 0.2)(B, H, Sk, Dh)),
            "wq": _w((lambda *s: torch.randn(*s, dtype=torch.bfloat16) * 1.6)(D, H * Dh) * (D ** -0.5)),
            "wo": _w((lambda *s: torch.randn(*s, dtype=torch.bfloat16) * 0.2)(H * Dh, D) * ((H * Dh) ** -0.5)),
        }
    _A_in = _A_setup()

    def _B_setup():
        B, Sq, D = 4, 1, 1024
        Hq, Hkv, Sk, Dh = 32, 4, 8192, 128
        assert Hq % Hkv == 0, (Hq, Hkv)
        return {
            "x": _w((lambda *s: torch.randn(*s, dtype=torch.bfloat16) * 0.8)(B, Sq, D)),
            "k_cache": _w((lambda *s: torch.randn(*s, dtype=torch.bfloat16) * 1.6)(B, Hkv, Sk, Dh)),
            "v_cache": _w((lambda *s: torch.randn(*s, dtype=torch.bfloat16) * 0.2)(B, Hkv, Sk, Dh)),
            "wq": _w((lambda *s: torch.randn(*s, dtype=torch.bfloat16) * 1.6)(D, Hq * Dh) * (D ** -0.5)),
            "wo": _w((lambda *s: torch.randn(*s, dtype=torch.bfloat16) * 0.2)(Hq * Dh, D) * ((Hq * Dh) ** -0.5)),
        }
    _B_in = _B_setup()

    return {
        "A_x": _A_in["x"],
        "A_k_cache": _A_in["k_cache"],
        "A_v_cache": _A_in["v_cache"],
        "A_wq": _A_in["wq"],
        "A_wo": _A_in["wo"],
        "B_x": _B_in["x"],
        "B_k_cache": _B_in["k_cache"],
        "B_v_cache": _B_in["v_cache"],
        "B_wq": _B_in["wq"],
        "B_wo": _B_in["wo"],
    }

def reference(A_x, A_k_cache, A_v_cache, A_wq, A_wo, B_x, B_k_cache, B_v_cache, B_wq, B_wo):
    def _A_ref(x, k_cache, v_cache, wq, wo):
        B, Sq, D = x.shape
        H, Dh = 32, 128
        q = (lambda x, *d: x.permute(*d))((x @ wq).reshape(B, Sq, H, Dh), 0, 2, 1, 3)
        scores = torch.einsum("bhqd,bhkd->bhqk", q, k_cache) * (Dh ** -0.5)
        p = _softmax(scores, axis=-1)
        o = torch.einsum("bhqk,bhkd->bhqd", p, v_cache)
        return (lambda x, *d: x.permute(*d))(o, 0, 2, 1, 3).reshape(B, Sq, H * Dh) @ wo

    def _B_ref(x, k_cache, v_cache, wq, wo):
        B, Sq, D = x.shape
        Hq, Dh = 32, 128
        Hkv, Sk = k_cache.shape[1], k_cache.shape[2]
        W = min(4096, max(256, Sk // 4))
        rep = Hq // Hkv
        q = (lambda x, *d: x.permute(*d))((x @ wq).reshape(B, Sq, Hq, Dh), 0, 2, 1, 3)
        k = (lambda x, n, axis: torch.repeat_interleave(x, n, dim=axis))(k_cache[:, :, Sk - W:, :], rep, axis=1)
        v = (lambda x, n, axis: torch.repeat_interleave(x, n, dim=axis))(v_cache[:, :, Sk - W:, :], rep, axis=1)
        scores = torch.einsum("bhqd,bhkd->bhqk", q, k) * (Dh ** -0.5)
        p = _softmax(scores, axis=-1)
        o = torch.einsum("bhqk,bhkd->bhqd", p, v)
        return (lambda x, *d: x.permute(*d))(o, 0, 2, 1, 3).reshape(B, Sq, Hq * Dh) @ wo

    a = _A_ref(A_x, A_k_cache, A_v_cache, A_wq, A_wo)
    b = _B_ref(B_x, B_k_cache, B_v_cache, B_wq, B_wo)
    return torch.concatenate([a, b], axis=-1)
```

```cpp
#include <hip/hip_runtime.h>
#include <hip/hip_bf16.h>
#include <cstdint>

typedef __attribute__((ext_vector_type(4))) unsigned u32x4;
typedef __attribute__((ext_vector_type(2))) __bf16 bf16x2;
typedef unsigned short u16;

constexpr int NB = 4, DM = 1024, NH = 32, DH = 128, SK = 8192, HKV = 4, WIN = 2048;
constexpr int HD = NH * DH;
constexpr int SPLIT_A = 8;
constexpr int SPLIT_B = 8;

__device__ __forceinline__ float bf2f(u16 v) { return __uint_as_float(((unsigned)v) << 16); }
__device__ __forceinline__ float dot2(unsigned k, unsigned q, float acc) {
  return __builtin_amdgcn_fdot2_f32_bf16(__builtin_bit_cast(bf16x2, k), __builtin_bit_cast(bf16x2, q), acc, false);
}
__device__ __forceinline__ u16 f2bf(float f) { return __builtin_bit_cast(u16, (__bf16)f); }

__global__ void __launch_bounds__(256) qproj(const u16* xA, const u16* wA, const u16* xB, const u16* wB, u16* q) {
  __shared__ float red[4][NB][64];
  const int br = blockIdx.y;
  const u16* x = br ? xB : xA; const u16* w = br ? wB : wA;
  const int lane = threadIdx.x & 63, wave = threadIdx.x >> 6;
  const int col = blockIdx.x * 64 + lane;
  float acc[NB] = {0.f, 0.f, 0.f, 0.f};
  for (int r = wave * 256; r < wave * 256 + 256; ++r) {
    float wv = bf2f(w[(size_t)r * HD + col]);
#pragma unroll
    for (int b = 0; b < NB; ++b) acc[b] = fmaf(bf2f(x[b * DM + r]), wv, acc[b]);
  }
#pragma unroll
  for (int b = 0; b < NB; ++b) red[wave][b][lane] = acc[b];
  __syncthreads();
  if (wave < NB) {
    float s = red[0][wave][lane] + red[1][wave][lane] + red[2][wave][lane] + red[3][wave][lane];
    q[(br * NB + wave) * HD + col] = f2bf(s);
  }
}

__global__ void __launch_bounds__(256) attn(const u16* q, const u16* kA, const u16* vA, const u16* kB, const u16* vB, float* part) {
  __shared__ float mrg[4][132];
  int id = blockIdx.x;
  const int br = id >= NB * NH * SPLIT_A ? 1 : 0;
  if (br) id -= NB * NH * SPLIT_A;
  const int nsplit = br ? SPLIT_B : SPLIT_A;
  const int s = id % nsplit, bh = id / nsplit, b = bh / NH, h = bh % NH;
  const int nkeys = br ? WIN / SPLIT_B : SK / SPLIT_A;
  const u16* kp = br ? kB + ((size_t)(b * HKV + h / 8) * SK + (SK - WIN) + (size_t)s * nkeys) * DH : kA + ((size_t)bh * SK + (size_t)s * nkeys) * DH;
  const u16* vp = br ? vB + ((size_t)(b * HKV + h / 8) * SK + (SK - WIN) + (size_t)s * nkeys) * DH : vA + ((size_t)bh * SK + (size_t)s * nkeys) * DH;
  const int lane = threadIdx.x & 63, wave = threadIdx.x >> 6, r = lane >> 4, c = lane & 15;
  const u32x4 qv = *(const u32x4*)(q + (br * NB + b) * HD + h * DH + 8 * c);
  const float C2 = 0.088388347648318440f * 1.4426950408889634f;
  float m = -INFINITY, l = 0.f, acc[8];
#pragma unroll
  for (int k = 0; k < 8; ++k) acc[k] = 0.f;
  for (int t0 = wave * 16; t0 < nkeys; t0 += 64) {
    u32x4 K[4], V[4];
#pragma unroll
    for (int t = 0; t < 4; ++t) {
      K[t] = __builtin_nontemporal_load((const u32x4*)(kp + (size_t)(t0 + 4 * t + r) * DH + 8 * c));
      V[t] = __builtin_nontemporal_load((const u32x4*)(vp + (size_t)(t0 + 4 * t + r) * DH + 8 * c));
    }
    float sc[4];
#pragma unroll
    for (int t = 0; t < 4; ++t) {
      float d = 0.f;
#pragma unroll
      for (int k = 0; k < 4; ++k) d = dot2(K[t][k], qv[k], d);
      d += __shfl_xor(d, 1); d += __shfl_xor(d, 2); d += __shfl_xor(d, 4); d += __shfl_xor(d, 8);
      sc[t] = d * C2;
    }
    float pm = fmaxf(fmaxf(sc[0], sc[1]), fmaxf(sc[2], sc[3]));
    float mn = fmaxf(m, pm);
    float al = exp2f(m - mn);
    m = mn; l *= al;
#pragma unroll
    for (int k = 0; k < 8; ++k) acc[k] *= al;
#pragma unroll
    for (int t = 0; t < 4; ++t) {
      float p = exp2f(sc[t] - mn); l += p;
#pragma unroll
      for (int k = 0; k < 4; ++k) {
        acc[2 * k] = fmaf(p, __uint_as_float(V[t][k] << 16), acc[2 * k]);
        acc[2 * k + 1] = fmaf(p, __uint_as_float(V[t][k] & 0xffff0000u), acc[2 * k + 1]);
      }
    }
  }
  float M = fmaxf(m, __shfl_xor(m, 16)); M = fmaxf(M, __shfl_xor(M, 32));
  float w = exp2f(m - M); l *= w;
  l += __shfl_xor(l, 16); l += __shfl_xor(l, 32);
#pragma unroll
  for (int k = 0; k < 8; ++k) { float a = acc[k] * w; a += __shfl_xor(a, 16); a += __shfl_xor(a, 32); acc[k] = a; }
  if (r == 0) {
#pragma unroll
    for (int k = 0; k < 8; ++k) mrg[wave][4 + 8 * c + k] = acc[k];
    if (c == 0) { mrg[wave][0] = M; mrg[wave][1] = l; }
  }
  __syncthreads();
  if (threadIdx.x < 128) {
    float MM = fmaxf(fmaxf(mrg[0][0], mrg[1][0]), fmaxf(mrg[2][0], mrg[3][0]));
    float L = 0.f, o = 0.f;
#pragma unroll
    for (int w4 = 0; w4 < 4; ++w4) { float wt = exp2f(mrg[w4][0] - MM); L += wt * mrg[w4][1]; o += wt * mrg[w4][4 + threadIdx.x]; }
    float* p = part + (size_t)blockIdx.x * 132;
    p[4 + threadIdx.x] = o;
    if (threadIdx.x == 0) { p[0] = MM; p[1] = L; }
  }
}

__global__ void __launch_bounds__(1024) outproj(const float* part, const u16* woA, const u16* woB, u16* out) {
  __shared__ float o[NB][HD];
  __shared__ float red[16][NB][64];
  const int br = blockIdx.y;
  const u16* wo = br ? woB : woA;
  const int nsplit = br ? SPLIT_B : SPLIT_A;
  const float* pb = part + (br ? (size_t)NB * NH * SPLIT_A * 132 : 0);
  for (int i = threadIdx.x; i < NB * HD; i += 1024) {
    int bh = i / DH, d = i % DH;
    const float* p = pb + (size_t)bh * nsplit * 132;
    float M = -INFINITY;
    for (int s = 0; s < nsplit; ++s) M = fmaxf(M, p[s * 132]);
    float L = 0.f, a = 0.f;
    for (int s = 0; s < nsplit; ++s) { float wt = exp2f(p[s * 132] - M); L += wt * p[s * 132 + 1]; a += wt * p[s * 132 + 4 + d]; }
    o[bh / NH][(bh % NH) * DH + d] = bf2f(f2bf(a / L));
  }
  __syncthreads();
  const int lane = threadIdx.x & 63, wave = threadIdx.x >> 6;
  const int col = blockIdx.x * 64 + lane;
  float acc[NB] = {0.f, 0.f, 0.f, 0.f};
  for (int r = wave * 256; r < wave * 256 + 256; ++r) {
    float wv = bf2f(wo[(size_t)r * DM + col]);
#pragma unroll
    for (int b = 0; b < NB; ++b) acc[b] = fmaf(o[b][r], wv, acc[b]);
  }
#pragma unroll
  for (int b = 0; b < NB; ++b) red[wave][b][lane] = acc[b];
  __syncthreads();
  if (wave < NB) {
    float s = 0.f;
    for (int w = 0; w < 16; ++w) s += red[w][wave][lane];
    out[wave * 2 * DM + br * DM + col] = f2bf(s);
  }
}

extern "C" void kernel_launch(void* const* d_in, const int* in_sizes, int n_in, void* d_out, int out_size, void* d_ws, size_t ws_size, hipStream_t stream) {
  const u16* xA = (const u16*)d_in[0]; const u16* kA = (const u16*)d_in[1]; const u16* vA = (const u16*)d_in[2];
  const u16* wqA = (const u16*)d_in[3]; const u16* woA = (const u16*)d_in[4];
  const u16* xB = (const u16*)d_in[5]; const u16* kB = (const u16*)d_in[6]; const u16* vB = (const u16*)d_in[7];
  const u16* wqB = (const u16*)d_in[8]; const u16* woB = (const u16*)d_in[9];
  u16* q = (u16*)d_ws;
  float* part = (float*)((char*)d_ws + 65536);
  qproj<<<dim3(HD / 64, 2), 256, 0, stream>>>(xA, wqA, xB, wqB, q);
  attn<<<NB * NH * (SPLIT_A + SPLIT_B), 256, 0, stream>>>(q, kA, vA, kB, vB, part);
  outproj<<<dim3(DM / 64, 2), 1024, 0, stream>>>(part, woA, woB, (u16*)d_out);
}
```

```cpp
#include <hip/hip_runtime.h>
#include <hip/hip_bf16.h>
#include <cstdint>

typedef __attribute__((ext_vector_type(4))) unsigned u32x4;
typedef __attribute__((ext_vector_type(4))) float f32x4;
typedef __attribute__((ext_vector_type(8))) __bf16 bf16x8;
typedef __attribute__((ext_vector_type(2))) __bf16 bf16x2;
typedef __attribute__((ext_vector_type(2))) float f32x2;
typedef unsigned short u16;
typedef unsigned long long u64;

constexpr int NB = 4, DM = 1024, NH = 32, DH = 128, SK = 8192, HKV = 4, WIN = 2048;
constexpr int HD = NH * DH;
constexpr int SPLIT_A = 2;
constexpr int SPLIT_B = 8;
constexpr float SCALE = 0.088388347648318440f, LOG2E = 1.4426950408889634f;
#define QK_MFMA 1
#define NTF 1
#define NSW 4

__device__ __forceinline__ float bf2f(u16 v) { return __uint_as_float(((unsigned)v) << 16); }
__device__ __forceinline__ float dot2(unsigned k, unsigned q, float acc) {
  return __builtin_amdgcn_fdot2_f32_bf16(__builtin_bit_cast(bf16x2, k), __builtin_bit_cast(bf16x2, q), acc, false);
}
__device__ __forceinline__ u16 f2bf(float f) { return __builtin_bit_cast(u16, (__bf16)f); }
__device__ __forceinline__ float bf16r(float f) { return bf2f(f2bf(f)); }
__device__ __forceinline__ unsigned pack2(float lo, float hi) { bf16x2 v = __builtin_convertvector(f32x2{lo, hi}, bf16x2); return __builtin_bit_cast(unsigned, v); }
template <int CTRL> __device__ __forceinline__ float dppf(float x) { return __builtin_bit_cast(float, __builtin_amdgcn_mov_dpp(__builtin_bit_cast(int, x), CTRL, 0xf, 0xf, true)); }
template <int CTRL> __device__ __forceinline__ unsigned dppu(unsigned x) { return (unsigned)__builtin_amdgcn_mov_dpp((int)x, CTRL, 0xf, 0xf, true); }
__device__ __forceinline__ float xrow_max(float x) {
  auto s = __builtin_amdgcn_permlane16_swap(__float_as_uint(x), __float_as_uint(x), false, false);
  x = fmaxf(__uint_as_float(s[0]), __uint_as_float(s[1]));
  auto t = __builtin_amdgcn_permlane32_swap(__float_as_uint(x), __float_as_uint(x), false, false);
  return fmaxf(__uint_as_float(t[0]), __uint_as_float(t[1]));
}
__device__ __forceinline__ float xrow_sum(float x) {
  auto s = __builtin_amdgcn_permlane16_swap(__float_as_uint(x), __float_as_uint(x), false, false);
  x = __uint_as_float(s[0]) + __uint_as_float(s[1]);
  auto t = __builtin_amdgcn_permlane32_swap(__float_as_uint(x), __float_as_uint(x), false, false);
  return __uint_as_float(t[0]) + __uint_as_float(t[1]);
}
__device__ __forceinline__ f32x4 mfma16(u32x4 a, u32x4 b, f32x4 c) {
  return __builtin_amdgcn_mfma_f32_16x16x32_bf16(__builtin_bit_cast(bf16x8, a), __builtin_bit_cast(bf16x8, b), c, 0, 0, 0);
}
template <int OFF> __device__ __forceinline__ void aload(u32x4& d, unsigned voff, const void* sbase) {
  asm volatile("global_load_dwordx4 %0, %1, %2 offset:%3 nt" : "+v"(d) : "v"(voff), "s"(sbase), "i"(OFF) : "memory");
}
template <int N> __device__ __forceinline__ void waitv(u32x4& d) { asm volatile("s_waitcnt vmcnt(%1)" : "+v"(d) : "i"(N) : "memory"); }

struct AttnState { f32x4 acc; float l, m, mL; };

template <bool RELOAD, int W0>
__device__ __forceinline__ void tile_step(AttnState& st, u32x4 (&K)[4], u32x4 (&V)[4], const u32x4 (&QB)[4], const unsigned (&msk)[4],
                                          unsigned voffK, unsigned voffV, const char* kn, const char* vn) {
  float d;
  f32x4 s = {0.f, 0.f, 0.f, 0.f};
  waitv<RELOAD ? W0 : W0 - 0>(K[0]); s = mfma16(K[0], QB[0], s); if (RELOAD) { __builtin_amdgcn_sched_barrier(0); aload<0>(K[0], voffK, kn); }
  waitv<RELOAD ? W0 : W0 - 1>(K[1]); s = mfma16(K[1], QB[1], s); if (RELOAD) { __builtin_amdgcn_sched_barrier(0); aload<1024>(K[1], voffK, kn); }
  waitv<RELOAD ? W0 : W0 - 2>(K[2]); s = mfma16(K[2], QB[2], s); if (RELOAD) { __builtin_amdgcn_sched_barrier(0); aload<2048>(K[2], voffK, kn); }
  waitv<RELOAD ? W0 : W0 - 3>(K[3]); s = mfma16(K[3], QB[3], s); if (RELOAD) { __builtin_amdgcn_sched_barrier(0); aload<3072>(K[3], voffK, kn); }
  d = dppf<0x00>(s[0]); d += dppf<0x55>(s[1]); d += dppf<0xAA>(s[2]); d += dppf<0xFF>(s[3]);
  const float s2 = bf16r(bf16r(d) * SCALE);
  if (__builtin_amdgcn_ballot_w64(s2 > st.m) != 0) {
    float mx = fmaxf(s2, dppf<0x124>(s2)); mx = fmaxf(mx, dppf<0x128>(mx)); mx = xrow_max(mx);
    mx = fmaxf(mx, st.m);
    const float mxL = mx * LOG2E;
    const float al = __builtin_amdgcn_exp2f(st.mL - mxL);
    st.acc *= al; st.l *= al; st.m = mx; st.mL = mxL;
  }
  const float p = __builtin_amdgcn_exp2f(__builtin_fmaf(s2, LOG2E, -st.mL));
  st.l += p;
  const unsigned pk = pack2(p, p);
#define PVI(i) { u32x4 B; B[0] = dppu<0x150 + 4 * i>(pk) & msk[0]; B[1] = dppu<0x150 + 4 * i>(pk) & msk[1]; B[2] = dppu<0x150 + 4 * i>(pk) & msk[2]; B[3] = dppu<0x150 + 4 * i>(pk) & msk[3]; \
    waitv<RELOAD ? W0 : W0 - 4 - i>(V[i]); st.acc = mfma16(V[i], B, st.acc); if (RELOAD) { __builtin_amdgcn_sched_barrier(0); aload<1024 * i>(V[i], voffV, vn); } }
  PVI(0) PVI(1) PVI(2) PVI(3)
#undef PVI
}

__global__ void __launch_bounds__(NSW * 64, 2) attnA(const u16* q, const u16* kA, const u16* vA, float* part, u64* stamps) {
  extern __shared__ __attribute__((aligned(16))) char smem[];
  float* mrg = (float*)smem;
  const int lane = threadIdx.x & 63, wave = __builtin_amdgcn_readfirstlane(threadIdx.x >> 6);
  u64 t0 = __builtin_amdgcn_s_memrealtime();
  const int bh = blockIdx.x >> 1, side = blockIdx.x & 1, b = bh / NH, h = bh % NH;
  const int g = lane >> 4, c = lane & 15;
  constexpr int RB = NSW * NTF * 4096;
  constexpr int NR = SK * DH * 2 / (2 * RB);
  const char* kb = (const char*)kA + (size_t)bh * SK * DH * 2 + side * RB + wave * NTF * 4096;
  const char* vb = (const char*)vA + (size_t)bh * SK * DH * 2 + side * RB + wave * NTF * 4096;
  const unsigned voffV = lane * 16;
  const unsigned voffK = ((lane >> 2) & 3) * 256 + g * 64 + (lane & 3) * 16;
  u32x4 QB[4]; unsigned msk[4];
  {
    const u32x4 qv = *(const u32x4*)(q + b * HD + h * DH + 8 * (4 * g + (lane & 3)));
#pragma unroll
    for (int i = 0; i < 4; ++i) QB[i] = ((c >> 2) == i) ? qv : u32x4{0u, 0u, 0u, 0u};
#pragma unroll
    for (int i = 0; i < 4; ++i) msk[i] = (c == 2 * i) ? 0x0000ffffu : (c == 2 * i + 1) ? 0xffff0000u : 0u;
    asm volatile("s_waitcnt vmcnt(0)" : "+v"(QB[0]), "+v"(QB[1]), "+v"(QB[2]), "+v"(QB[3]) :: "memory");
  }
  u32x4 K[NTF][4], V[NTF][4];
#pragma unroll
  for (int t = 0; t < NTF; ++t)
#pragma unroll
    for (int i = 0; i < 4; ++i) { K[t][i] = u32x4{0u, 0u, 0u, 0u}; V[t][i] = u32x4{0u, 0u, 0u, 0u}; }
#pragma unroll
  for (int t = 0; t < NTF; ++t) {
    aload<0>(K[t][0], voffK, kb + t * 4096); aload<1024>(K[t][1], voffK, kb + t * 4096); aload<2048>(K[t][2], voffK, kb + t * 4096); aload<3072>(K[t][3], voffK, kb + t * 4096);
    aload<0>(V[t][0], voffV, vb + t * 4096); aload<1024>(V[t][1], voffV, vb + t * 4096); aload<2048>(V[t][2], voffV, vb + t * 4096); aload<3072>(V[t][3], voffV, vb + t * 4096);
  }
  AttnState st; st.acc = f32x4{0.f, 0.f, 0.f, 0.f}; st.l = 0.f; st.m = -INFINITY; st.mL = -INFINITY;
#pragma unroll 1
  for (int r = 1; r < NR; ++r) {
    const size_t off = (size_t)r * 2 * RB;
#pragma unroll
    for (int t = 0; t < NTF; ++t) tile_step<true, 8 * NTF - 1>(st, K[t], V[t], QB, msk, voffK, voffV, kb + off + t * 4096, vb + off + t * 4096);
  }
  if (NTF == 1) tile_step<false, 7>(st, K[0], V[0], QB, msk, voffK, voffV, nullptr, nullptr);
  if (NTF == 2) { tile_step<false, 15>(st, K[0], V[0], QB, msk, voffK, voffV, nullptr, nullptr); tile_step<false, 7>(st, K[NTF - 1], V[NTF - 1], QB, msk, voffK, voffV, nullptr, nullptr); }
  float l = st.l; l += dppf<0x124>(l); l += dppf<0x128>(l); l = xrow_sum(l);
  if (c < 8) {
#pragma unroll
    for (int r = 0; r < 4; ++r) mrg[wave * 132 + 4 + 8 * (4 * g + r) + c] = st.acc[r];
  }
  if (lane == 0) { mrg[wave * 132] = st.mL; mrg[wave * 132 + 1] = l; }
  __syncthreads();
  if (threadIdx.x < 128) {
    float MM = mrg[0];
#pragma unroll
    for (int w = 1; w < NSW; ++w) MM = fmaxf(MM, mrg[w * 132]);
    float L = 0.f, o = 0.f;
#pragma unroll
    for (int w = 0; w < NSW; ++w) { float wt = __builtin_amdgcn_exp2f(mrg[w * 132] - MM); L += wt * mrg[w * 132 + 1]; o += wt * mrg[w * 132 + 4 + threadIdx.x]; }
    float* p = part + (size_t)blockIdx.x * 132;
    p[4 + threadIdx.x] = o;
    if (threadIdx.x == 0) { p[0] = MM; p[1] = L; }
  }
  if (stamps && threadIdx.x == 0) { stamps[2 * blockIdx.x] = t0; stamps[2 * blockIdx.x + 1] = __builtin_amdgcn_s_memrealtime(); }
}

__global__ void __launch_bounds__(256) qproj(const u16* xA, const u16* wA, const u16* xB, const u16* wB, u16* q) {
  __shared__ float red[4][NB][64];
  const int br = blockIdx.y;
  const u16* x = br ? xB : xA; const u16* w = br ? wB : wA;
  const int lane = threadIdx.x & 63, wave = threadIdx.x >> 6;
  const int col = blockIdx.x * 64 + lane;
  float acc[NB] = {0.f, 0.f, 0.f, 0.f};
  for (int r = wave * 256; r < wave * 256 + 256; ++r) {
    float wv = bf2f(w[(size_t)r * HD + col]);
#pragma unroll
    for (int b = 0; b < NB; ++b) acc[b] = fmaf(bf2f(x[b * DM + r]), wv, acc[b]);
  }
#pragma unroll
  for (int b = 0; b < NB; ++b) red[wave][b][lane] = acc[b];
  __syncthreads();
  if (wave < NB) {
    float s = red[0][wave][lane] + red[1][wave][lane] + red[2][wave][lane] + red[3][wave][lane];
    q[(br * NB + wave) * HD + col] = f2bf(s);
  }
}

__global__ void __launch_bounds__(256) attnB(const u16* q, const u16* kB, const u16* vB, float* part) {
  __shared__ float mrg[4][132];
  int id = blockIdx.x;
  const int s = id % SPLIT_B, bh = id / SPLIT_B, b = bh / NH, h = bh % NH;
  const int nkeys = WIN / SPLIT_B;
  const u16* kp = kB + ((size_t)(b * HKV + h / 8) * SK + (SK - WIN) + (size_t)s * nkeys) * DH;
  const u16* vp = vB + ((size_t)(b * HKV + h / 8) * SK + (SK - WIN) + (size_t)s * nkeys) * DH;
  const int lane = threadIdx.x & 63, wave = threadIdx.x >> 6, r = lane >> 4, c = lane & 15;
  const u32x4 qv = *(const u32x4*)(q + (NB + b) * HD + h * DH + 8 * c);
  float m = -INFINITY, l = 0.f, acc[8];
#pragma unroll
  for (int k = 0; k < 8; ++k) acc[k] = 0.f;
  for (int t0 = wave * 16; t0 < nkeys; t0 += 64) {
    u32x4 K[4], V[4];
#pragma unroll
    for (int t = 0; t < 4; ++t) {
      K[t] = *((const u32x4*)(kp + (size_t)(t0 + 4 * t + r) * DH + 8 * c));
      V[t] = *((const u32x4*)(vp + (size_t)(t0 + 4 * t + r) * DH + 8 * c));
    }
    float sc[4];
#pragma unroll
    for (int t = 0; t < 4; ++t) {
      float d = 0.f;
#pragma unroll
      for (int k = 0; k < 4; ++k) d = dot2(K[t][k], qv[k], d);
      d += __shfl_xor(d, 1); d += __shfl_xor(d, 2); d += __shfl_xor(d, 4); d += __shfl_xor(d, 8);
      sc[t] = bf16r(bf16r(d) * SCALE) * LOG2E;
    }
    float pm = fmaxf(fmaxf(sc[0], sc[1]), fmaxf(sc[2], sc[3]));
    float mn = fmaxf(m, pm);
    float al = exp2f(m - mn);
    m = mn; l *= al;
#pragma unroll
    for (int k = 0; k < 8; ++k) acc[k] *= al;
#pragma unroll
    for (int t = 0; t < 4; ++t) {
      float p = exp2f(sc[t] - mn); l += p;
#pragma unroll
      for (int k = 0; k < 4; ++k) {
        acc[2 * k] = fmaf(p, __uint_as_float(V[t][k] << 16), acc[2 * k]);
        acc[2 * k + 1] = fmaf(p, __uint_as_float(V[t][k] & 0xffff0000u), acc[2 * k + 1]);
      }
    }
  }
  float M = fmaxf(m, __shfl_xor(m, 16)); M = fmaxf(M, __shfl_xor(M, 32));
  float w = exp2f(m - M); l *= w;
  l += __shfl_xor(l, 16); l += __shfl_xor(l, 32);
#pragma unroll
  for (int k = 0; k < 8; ++k) { float a = acc[k] * w; a += __shfl_xor(a, 16); a += __shfl_xor(a, 32); acc[k] = a; }
  if (r == 0) {
#pragma unroll
    for (int k = 0; k < 8; ++k) mrg[wave][4 + 8 * c + k] = acc[k];
    if (c == 0) { mrg[wave][0] = M; mrg[wave][1] = l; }
  }
  __syncthreads();
  if (threadIdx.x < 128) {
    float MM = fmaxf(fmaxf(mrg[0][0], mrg[1][0]), fmaxf(mrg[2][0], mrg[3][0]));
    float L = 0.f, o = 0.f;
#pragma unroll
    for (int w4 = 0; w4 < 4; ++w4) { float wt = exp2f(mrg[w4][0] - MM); L += wt * mrg[w4][1]; o += wt * mrg[w4][4 + threadIdx.x]; }
    float* p = part + (size_t)blockIdx.x * 132;
    p[4 + threadIdx.x] = o;
    if (threadIdx.x == 0) { p[0] = MM; p[1] = L; }
  }
}

__global__ void __launch_bounds__(1024) outproj(const float* part, const u16* woA, const u16* woB, u16* out) {
  __shared__ float o[NB][HD];
  __shared__ float red[16][NB][64];
  const int br = blockIdx.y;
  const u16* wo = br ? woB : woA;
  const int nsplit = br ? SPLIT_B : SPLIT_A;
  const float* pb = part + (br ? (size_t)NB * NH * SPLIT_A * 132 : 0);
  for (int i = threadIdx.x; i < NB * HD; i += 1024) {
    int bh = i / DH, d = i % DH;
    const float* p = pb + (size_t)bh * nsplit * 132;
    float M = -INFINITY;
    for (int s = 0; s < nsplit; ++s) M = fmaxf(M, p[s * 132]);
    float L = 0.f, a = 0.f;
    for (int s = 0; s < nsplit; ++s) { float wt = exp2f(p[s * 132] - M); L += wt * p[s * 132 + 1]; a += wt * p[s * 132 + 4 + d]; }
    o[bh / NH][(bh % NH) * DH + d] = bf2f(f2bf(a / L));
  }
  __syncthreads();
  const int lane = threadIdx.x & 63, wave = threadIdx.x >> 6;
  const int col = blockIdx.x * 64 + lane;
  float acc[NB] = {0.f, 0.f, 0.f, 0.f};
  for (int r = wave * 256; r < wave * 256 + 256; ++r) {
    float wv = bf2f(wo[(size_t)r * DM + col]);
#pragma unroll
    for (int b = 0; b < NB; ++b) acc[b] = fmaf(o[b][r], wv, acc[b]);
  }
#pragma unroll
  for (int b = 0; b < NB; ++b) red[wave][b][lane] = acc[b];
  __syncthreads();
  if (wave < NB) {
    float s = 0.f;
    for (int w = 0; w < 16; ++w) s += red[w][wave][lane];
    out[wave * 2 * DM + br * DM + col] = f2bf(s);
  }
}

extern "C" void kernel_launch(void* const* d_in, const int* in_sizes, int n_in, void* d_out, int out_size, void* d_ws, size_t ws_size, hipStream_t stream) {
  const u16* xA = (const u16*)d_in[0]; const u16* kA = (const u16*)d_in[1]; const u16* vA = (const u16*)d_in[2];
  const u16* wqA = (const u16*)d_in[3]; const u16* woA = (const u16*)d_in[4];
  const u16* xB = (const u16*)d_in[5]; const u16* kB = (const u16*)d_in[6]; const u16* vB = (const u16*)d_in[7];
  const u16* wqB = (const u16*)d_in[8]; const u16* woB = (const u16*)d_in[9];
  u16* q = (u16*)d_ws;
  float* part = (float*)((char*)d_ws + 65536);
  u64* stamps = (u64*)((char*)d_ws + (64 << 20));
  (void)hipFuncSetAttribute((const void*)attnA, hipFuncAttributeMaxDynamicSharedMemorySize, 160 * 1024);
  qproj<<<dim3(HD / 64, 2), 256, 0, stream>>>(xA, wqA, xB, wqB, q);
  attnA<<<256, NSW * 64, 90000, stream>>>(q, kA, vA, part, stamps);
  attnB<<<NB * NH * SPLIT_B, 256, 0, stream>>>(q, kB, vB, part + (size_t)NB * NH * SPLIT_A * 132);
  outproj<<<dim3(DM / 64, 2), 1024, 0, stream>>>(part, woA, woB, (u16*)d_out);
}
```

```cpp
#include <hip/hip_runtime.h>
#include <hip/hip_bf16.h>
#include <cstdint>
#pragma clang diagnostic ignored "-Wint-to-pointer-cast"

typedef __attribute__((ext_vector_type(4))) unsigned u32x4;
typedef __attribute__((ext_vector_type(2))) unsigned u32x2;
typedef __attribute__((ext_vector_type(4))) float f32x4;
typedef __attribute__((ext_vector_type(2))) float f32x2;
typedef __attribute__((ext_vector_type(8))) __bf16 bf16x8;
typedef __attribute__((ext_vector_type(2))) __bf16 bf16x2;
typedef __attribute__((ext_vector_type(4))) short s16x4;
typedef unsigned short u16;
typedef unsigned long long u64;
#define LAS __attribute__((address_space(3)))
#define GAS __attribute__((address_space(1)))
#define INL __device__ __forceinline__

constexpr int DM = 1024, NH = 32, DH = 128, SK = 8192, WIN = 2048, HD = NH * DH;
constexpr float SCALE = 0.088388347648318440f, LOG2E = 1.4426950408889634f;
constexpr int NWG = 256, NTHR = 512;

#define R0 56
#define QA_SLEEP 8
#define B_SLEEP 64
#define PA_SLEEP 2
#define Y_SLEEP 1
#define DRY 0
#define LATCH 1
#define XPAIR 3

constexpr unsigned WS_EPOCH = 0;
constexpr unsigned WS_OCT = 4096;
constexpr unsigned WS_QA1 = WS_OCT + 32 * 128;
constexpr unsigned PA_REC = 1152;
constexpr unsigned WS_PA = WS_QA1 + NWG * 2048;
constexpr unsigned WS_YA = WS_PA + NWG * PA_REC;
constexpr unsigned WS_CTR = WS_YA + NWG * 2048;
constexpr unsigned WS_QB1 = WS_CTR + 128 * 256;
constexpr unsigned WS_QB2 = WS_QB1 + NWG * 1024;
constexpr unsigned WS_PB = WS_QB2 + 128 * 256;
constexpr unsigned WS_PBML = WS_PB + 128 * 16 * 512;
constexpr unsigned WS_OB = WS_PBML + 128 * 16 * 8;
constexpr unsigned WS_YB = WS_OB + 128 * 256;
constexpr unsigned WS_FQ = WS_YB + 64 * 4096;
constexpr unsigned WS_FQ2 = WS_FQ + NWG * 32;
constexpr unsigned WS_FP = WS_FQ2 + 64 * 8;
constexpr unsigned WS_FO = WS_FP + NWG * 32;
constexpr unsigned WS_FY = WS_FO + NWG * 8;
constexpr unsigned WS_END = WS_FY + 64 * 128;
static_assert(WS_END <= (4u << 20), "workspace");

constexpr unsigned L_R0 = 0, L_R1 = 32768, L_KB = 65536, L_VB = 98304;
constexpr unsigned L_QA = 131072;
constexpr unsigned L_QB = L_QA + 256;
constexpr unsigned L_OA = L_QB + 8 * 272;
constexpr unsigned L_OB = L_OA + 4 * 576;
constexpr unsigned L_PART = L_OB + 4 * 576;
constexpr unsigned L_X = L_PART + 4 * 528;
constexpr unsigned L_CNT = L_X + 4096;
constexpr unsigned LDS_BYTES = L_CNT + 64 + 256;
enum { C_WQA = 0, C_WQB, C_KV, C_FREE0, C_FREE1, C_QA, C_WOB, C_QB, C_OB, C_X, C_ROLE };
constexpr unsigned SPIN_CAP = 1u << 17;

#define STAMP(i) do { } while (0)

struct Params {
  const u16 *xA, *kA, *vA, *wqA, *woA, *xB, *kB, *vB, *wqB, *woB;
  u16* out;
  unsigned char* ws;
};

template <int V> struct IC { static constexpr int value = V; };
template <int A, int B, typename F> INL void static_for(F&& f) { if constexpr (A < B) { f(IC<A>{}); static_for<A + 1, B>(f); } }
INL float bf2f(u16 v) { return __uint_as_float(((unsigned)v) << 16); }
INL u16 f2bf(float f) { return __builtin_bit_cast(u16, (__bf16)f); }
INL float bf16r(float f) { return bf2f(f2bf(f)); }
INL unsigned pack2(float lo, float hi) { return __builtin_bit_cast(unsigned, __builtin_convertvector(f32x2{lo, hi}, bf16x2)); }
template <int CTRL> INL float dppf(float x) { return __builtin_bit_cast(float, __builtin_amdgcn_mov_dpp(__builtin_bit_cast(int, x), CTRL, 0xf, 0xf, true)); }
template <int CTRL> INL unsigned dppu(unsigned x) { return (unsigned)__builtin_amdgcn_mov_dpp((int)x, CTRL, 0xf, 0xf, true); }
constexpr int XOR1 = 0xB1, XOR2 = 0x4E, ROR4 = 0x124, ROR8 = 0x128, BCAST = 0x150;
INL float x16_max(float x) { auto s = __builtin_amdgcn_permlane16_swap(__float_as_uint(x), __float_as_uint(x), false, false); return fmaxf(__uint_as_float(s[0]), __uint_as_float(s[1])); }
INL float x32_max(float x) { auto s = __builtin_amdgcn_permlane32_swap(__float_as_uint(x), __float_as_uint(x), false, false); return fmaxf(__uint_as_float(s[0]), __uint_as_float(s[1])); }
INL float x16_sum(float x) { auto s = __builtin_amdgcn_permlane16_swap(__float_as_uint(x), __float_as_uint(x), false, false); return __uint_as_float(s[0]) + __uint_as_float(s[1]); }
INL float x32_sum(float x) { auto s = __builtin_amdgcn_permlane32_swap(__float_as_uint(x), __float_as_uint(x), false, false); return __uint_as_float(s[0]) + __uint_as_float(s[1]); }
INL f32x4 mfma16(u32x4 a, u32x4 b, f32x4 c) { return __builtin_amdgcn_mfma_f32_16x16x32_bf16(__builtin_bit_cast(bf16x8, a), __builtin_bit_cast(bf16x8, b), c, 0, 0, 0); }
INL float ex2(float x) { return __builtin_amdgcn_exp2f(x); }
INL float mulL(float m) { float r = m * LOG2E; asm volatile("" : "+v"(r)); return r; }
INL u32x4 asu(const f32x4& v) { return __builtin_bit_cast(u32x4, v); }
INL f32x4 asf(const u32x4& v) { return __builtin_bit_cast(f32x4, v); }
INL float sel4(const f32x4& d, int g) { return (g & 2) ? ((g & 1) ? d[3] : d[2]) : ((g & 1) ? d[1] : d[0]); }
template <typename T> INL void pin(T& d) { asm volatile("" : "+v"(d)); }

INL u32x4 lds128(unsigned a) { return *(const LAS u32x4*)a; }
INL f32x2 lds64f(unsigned a) { return *(const LAS f32x2*)a; }
INL u32x2 lds_tr(unsigned a) { return __builtin_bit_cast(u32x2, __builtin_amdgcn_ds_read_tr16_b64_v4i16((LAS s16x4*)a)); }
INL void lds_signal(int c, unsigned n = 1) {
  u64 keep;
  asm volatile("s_waitcnt lgkmcnt(0)\n\ts_mov_b64 %0, exec\n\ts_mov_b64 exec, 1\n\tds_add_u32 %1, %2\n\ts_mov_b64 exec, %0" : "=&s"(keep) : "v"(L_CNT + 4 * c), "v"(n) : "memory");
}
INL unsigned lds_wait(int c, unsigned n) {
  unsigned v = 0;
#pragma unroll 1
  for (unsigned s = 0; s < SPIN_CAP * 8; ++s) {
    v = __builtin_amdgcn_readfirstlane(*(volatile LAS unsigned*)(L_CNT + 4 * c));
    if (v >= n) break;
    __builtin_amdgcn_s_sleep(1);
  }
  asm volatile("" ::: "memory");
  return v;
}

template <int OFF> INL void aload(u32x4& d, unsigned voff, const void* sbase) {
  asm volatile("global_load_dwordx4 %0, %1, %2 offset:%3 nt" : "+v"(d) : "v"(voff), "s"(sbase), "i"(OFF) : "memory");
}
template <int N> INL void waitv(u32x4& d) { asm volatile("s_waitcnt vmcnt(%1)" : "+v"(d) : "i"(N) : "memory"); }
template <int N> INL void waitvm() { asm volatile("s_waitcnt vmcnt(%0)" :: "i"(N) : "memory"); }
INL void claim_issue(unsigned& cl, const unsigned char* ctr) {
  u64 keep;
  asm volatile("s_mov_b64 %0, exec\n\ts_mov_b64 exec, 1\n\tglobal_atomic_add %1, %2, %3, %4 sc0 sc1\n\ts_mov_b64 exec, %0"
               : "=&s"(keep), "+v"(cl) : "v"(0u), "v"(1u), "s"(ctr) : "memory");
}
template <int W> INL unsigned claim_read(unsigned& cl) {
  unsigned got;
  asm volatile("s_waitcnt vmcnt(%2)\n\tv_readfirstlane_b32 %0, %1" : "=s"(got), "+v"(cl) : "i"(W) : "memory");
  return got;
}

template <bool NT> INL void st16(const unsigned char* ws, unsigned off, const u32x4& v) {
  if (NT) asm volatile("global_store_dwordx4 %0, %1, %2 sc1 nt\n\ts_nop 1" :: "v"(off), "v"(v), "s"(ws) : "memory");
  else asm volatile("global_store_dwordx4 %0, %1, %2 sc1\n\ts_nop 1" :: "v"(off), "v"(v), "s"(ws) : "memory");
}
INL void st8(const unsigned char* ws, unsigned off, const u32x2& v) { asm volatile("global_store_dwordx2 %0, %1, %2 sc1" :: "v"(off), "v"(v), "s"(ws) : "memory"); }
template <bool NT> INL void ld16(u32x4& d, const unsigned char* ws, unsigned off) {
  if (NT) asm volatile("global_load_dwordx4 %0, %1, %2 sc1 nt" : "+v"(d) : "v"(off), "s"(ws) : "memory");
  else asm volatile("global_load_dwordx4 %0, %1, %2 sc1" : "+v"(d) : "v"(off), "s"(ws) : "memory");
}
INL void ld8(u32x2& d, const unsigned char* ws, unsigned off) { asm volatile("global_load_dwordx2 %0, %1, %2 sc1" : "+v"(d) : "v"(off), "s"(ws) : "memory"); }
template <bool NT, int N> INL void fetch(u32x4 (&v)[N], const unsigned char* ws, unsigned off, unsigned stride) {
#pragma unroll
  for (int i = 0; i < N; ++i) { v[i] = u32x4{0u, 0u, 0u, 0u}; ld16<NT>(v[i], ws, off + i * stride); }
  waitvm<0>();
#pragma unroll
  for (int i = 0; i < N; ++i) pin(v[i]);
}

struct Tag { unsigned hi, f; };
INL float dec(unsigned lo) { return __uint_as_float(lo); }
INL void put2(const unsigned char* ws, unsigned off, float a, float b, const Tag& t, u64 mask) {
  u64 keep;
  asm volatile("s_mov_b64 %0, exec\n\ts_mov_b64 exec, %4\n\tglobal_store_dwordx4 %1, %2, %3 sc1\n\ts_mov_b64 exec, %0\n\ts_nop 0"
               : "=&s"(keep) : "v"(off), "v"(u32x4{__float_as_uint(a), t.hi, __float_as_uint(b), t.hi}), "s"(ws), "s"(mask) : "memory");
}
INL void ld16m(u32x4& d, const unsigned char* ws, unsigned off, u64 mask) {
  asm volatile("s_mov_b64 exec, %3\n\tglobal_load_dwordx4 %0, %1, %2 sc1\n\ts_mov_b64 exec, -1" : "+v"(d) : "v"(off), "s"(ws), "s"(mask) : "memory");
}
template <int N, int SLEEP> INL void gather16(u32x4 (&v)[N], const unsigned char* ws, const unsigned (&off)[N], const Tag& t, u64 on = ~0ull) {
  u64 need[N];
#pragma unroll
  for (int i = 0; i < N; ++i) { asm volatile("" : "=v"(v[i])); need[i] = on; }
#pragma unroll 1
  for (unsigned s = 0; s < SPIN_CAP; ++s) {
#pragma unroll
    for (int i = 0; i < N; ++i) ld16m(v[i], ws, off[i], need[i]);
    waitvm<0>();
    unsigned bad[N], any = 0;
#pragma unroll
    for (int i = 0; i < N; ++i) { pin(v[i]); bad[i] = (v[i][1] ^ t.hi) | (v[i][3] ^ t.hi); pin(bad[i]); any |= bad[i]; }
    if ((__builtin_amdgcn_ballot_w64(any != 0) & on) == 0) break;
#pragma unroll
    for (int i = 0; i < N; ++i) {
      const u64 m = __builtin_amdgcn_ballot_w64(bad[i] != 0);
      asm volatile("s_cmp_lg_u64 %1, 0\n\ts_cselect_b64 %0, -1, 0" : "=s"(need[i]) : "s"(m) : "scc");
    }
    if (SLEEP) __builtin_amdgcn_s_sleep(SLEEP);
  }
}
INL void raise_flag(const unsigned char* ws, unsigned off, const Tag& t) {
  waitvm<0>();
  if ((threadIdx.x & 63) == 0) st8(ws, off, u32x2{t.f, t.hi});
}
INL void wait_flags(const unsigned char* ws, unsigned off, const Tag& t) {
  u32x2 v = {0u, 0u};
#pragma unroll 1
  for (unsigned s = 0; s < SPIN_CAP; ++s) {
    __builtin_amdgcn_s_sleep(B_SLEEP);
    ld8(v, ws, off); waitvm<0>(); pin(v);
    if (__builtin_amdgcn_ballot_w64(((v[0] ^ t.f) | (v[1] ^ t.hi)) != 0) == 0) break;
  }
}

struct AttnState { f32x4 acc; float l, m, mL; };
template <bool RELOAD, int W>
INL void tile_step(AttnState& st, u32x4 (&K)[4], u32x4 (&V)[4], const u32x4 (&QB)[4], const unsigned (&msk)[4],
                   unsigned voffK, unsigned voffV, const char* kn, const char* vn) {
  f32x4 s = {0.f, 0.f, 0.f, 0.f};
#define QKI(i) waitv<RELOAD ? W : W - i>(K[i]); s = mfma16(K[i], QB[i], s); if (RELOAD) { __builtin_amdgcn_sched_barrier(0); aload<1024 * i>(K[i], voffK, kn); }
  QKI(0) QKI(1) QKI(2) QKI(3)
#undef QKI
  float d = dppf<0x00>(s[0]); d += dppf<0x55>(s[1]); d += dppf<0xAA>(s[2]); d += dppf<0xFF>(s[3]);
  const float s2 = bf16r(bf16r(d) * SCALE);
  if (__builtin_amdgcn_ballot_w64(s2 > st.m) != 0) {
    float mx = fmaxf(s2, dppf<ROR4>(s2)); mx = fmaxf(mx, dppf<ROR8>(mx)); mx = x32_max(x16_max(mx));
    mx = fmaxf(mx, st.m);
    const float mxL = mulL(mx), al = ex2(st.mL - mxL);
    st.acc *= al; st.l *= al; st.m = mx; st.mL = mxL;
  }
  const float pr = ex2(__builtin_fmaf(s2, LOG2E, -st.mL));
  st.l += pr;
  const unsigned pk = pack2(pr, pr);
#define PVI(i) { u32x4 B; B[0] = dppu<BCAST + 4 * i>(pk) & msk[0]; B[1] = dppu<BCAST + 4 * i>(pk) & msk[1]; B[2] = dppu<BCAST + 4 * i>(pk) & msk[2]; B[3] = dppu<BCAST + 4 * i>(pk) & msk[3]; \
    waitv<RELOAD ? W : W - 4 - i>(V[i]); st.acc = mfma16(V[i], B, st.acc); if (RELOAD) { __builtin_amdgcn_sched_barrier(0); aload<1024 * i>(V[i], voffV, vn); } }
  PVI(0) PVI(1) PVI(2) PVI(3)
#undef PVI
}

INL void load_wfrag(u32x4 (&B)[8], unsigned region, int w, int lane) {
  const int g = lane >> 4, q = (lane >> 2) & 3, pp = lane & 3;
  const unsigned sw = 2u * ((q >> 1) | ((g & 1) << 1));
  const unsigned a = region + (8 * g + q) * 128 + 16 * ((2 * w + (pp >> 1)) ^ sw) + 8 * (pp & 1);
#pragma unroll
  for (int kk = 0; kk < 8; ++kk) { const u32x2 lo = lds_tr(a + kk * 4096), hi = lds_tr(a + kk * 4096 + 512); B[kk] = u32x4{lo[0], lo[1], hi[0], hi[1]}; }
}
INL f32x4 piece(unsigned vec, unsigned vstride, int lane, const u32x4 (&B)[8]) {
  const unsigned a = vec + (lane & 3) * vstride + 16 * (lane >> 4);
  u32x4 A[8];
#pragma unroll
  for (int kk = 0; kk < 8; ++kk) A[kk] = lds128(a + 64 * kk);
  __builtin_amdgcn_sched_barrier(0);
  f32x4 d0 = {0.f, 0.f, 0.f, 0.f}, d1 = {0.f, 0.f, 0.f, 0.f};
#pragma unroll
  for (int kk = 0; kk < 8; kk += 2) { d0 = mfma16(A[kk], B[kk], d0); d1 = mfma16(A[kk + 1], B[kk + 1], d1); }
  return d0 + d1;
}
INL unsigned wtile_voff(int lane, int odd, unsigned rs) {
  const unsigned sw = 2u * (((lane >> 4) & 1) | (odd << 1));
  return (lane >> 3) * rs + 16 * ((lane & 7) ^ sw);
}

__global__ void __launch_bounds__(NTHR, 2) fused_decode(Params p) {
  const int lane = threadIdx.x & 63, wave = __builtin_amdgcn_readfirstlane(threadIdx.x >> 6);
  const int j = blockIdx.x, g = lane >> 4, c = lane & 15;

  if (wave < 4) {
    if (wave == 0) STAMP(0);
    const unsigned vq0 = wtile_voff(lane, 0, HD * 2), vq1 = wtile_voff(lane, 1, HD * 2);
    const unsigned vo0 = wtile_voff(lane, 0, DM * 2), vo1 = wtile_voff(lane, 1, DM * 2);
    const unsigned vkv = g * 256 + 16 * (c ^ (g << 2));
    const size_t qoff = ((size_t)((j & 3) * 256 + wave * 64) * HD + (j >> 2) * 64) * 2;
    const size_t ooff = ((size_t)((j >> 4) * 256 + wave * 64) * DM + (j & 15) * 64) * 2;
    const size_t boff = ((size_t)(j >> 4) * SK + (SK - WIN) + (j & 15) * 128 + wave * 32) * DH * 2;
    const unsigned voffV = lane * 16, voffK = ((lane >> 2) & 3) * 256 + g * 64 + (lane & 3) * 16;
    const unsigned ldst = wave * 8192 + lane * 16;
    u32x4 K[4], V[4];
#pragma unroll
    for (int i = 0; i < 4; ++i) { K[i] = u32x4{0u, 0u, 0u, 0u}; V[i] = u32x4{0u, 0u, 0u, 0u}; }
    int ja = 0; const char *ku = nullptr, *vu = nullptr;
    static_for<0, 56>([&](auto nc) __attribute__((always_inline)) {
      constexpr int n = decltype(nc)::value, k = n >> 3, i = n & 7;
      u32x4& R = i < 4 ? K[i & 3] : V[i & 3];
      if constexpr (n == 8) __builtin_amdgcn_s_barrier();
      if constexpr (n == 40) lds_wait(C_FREE0, 4);
      if constexpr (n == 48) {
        lds_wait(C_FREE1, 4);
        ja = (j & ~7) | (lds_wait(C_ROLE, 8) & 7);
        ku = (const char*)p.kA + (size_t)(ja >> 1) * SK * DH * 2; vu = (const char*)p.vA + (size_t)(ja >> 1) * SK * DH * 2;
      }
      if constexpr (n >= 8) {
        constexpr unsigned reg = k == 1 ? L_R0 : k == 2 ? L_R1 : k == 3 ? L_KB : k == 4 ? L_VB : k == 5 ? L_R0 : L_R1;
        asm volatile("s_waitcnt vmcnt(7)\n\tds_write_b128 %1, %0 offset:%2" : "+v"(R) : "v"(ldst + (reg & ~0xffffu)), "i"((reg & 0xffffu) + i * 1024) : "memory");
      }
      if constexpr (k == 0) aload<0>(R, (i & 1) ? vq1 : vq0, (const char*)p.wqA + qoff + (size_t)i * 8 * HD * 2);
      if constexpr (k == 1) aload<0>(R, (i & 1) ? vq1 : vq0, (const char*)p.wqB + qoff + (size_t)i * 8 * HD * 2);
      if constexpr (k == 2) aload<(i & 3) * 1024>(R, vkv, (const char*)p.kB + boff + (i >> 2) * 4096);
      if constexpr (k == 3) aload<(i & 3) * 1024>(R, vkv, (const char*)p.vB + boff + (i >> 2) * 4096);
      if constexpr (k == 4) aload<0>(R, (i & 1) ? vo1 : vo0, (const char*)p.woA + ooff + (size_t)i * 8 * DM * 2);
      if constexpr (k == 5) aload<0>(R, (i & 1) ? vo1 : vo0, (const char*)p.woB + ooff + (size_t)i * 8 * DM * 2);
      if constexpr (k == 6) aload<(i & 3) * 1024>(R, i < 4 ? voffK : voffV, (i < 4 ? ku : vu) + (4 * (ja & 1) + wave) * 4096);
      if constexpr (n == 15) lds_signal(C_WQA);
      if constexpr (n == 23) lds_signal(C_WQB);
      if constexpr (n == 39) lds_signal(C_KV);
      if constexpr (n == 55) lds_signal(C_WOB);
    });
    if (wave == 0) STAMP(1);
    lds_wait(C_QA, 1);
    if (wave == 0) STAMP(2);
    u32x4 QB[4]; unsigned msk[4];
    {
      const u32x4 qv = lds128(L_QA + 16 * (4 * g + (lane & 3)));
#pragma unroll
      for (int i = 0; i < 4; ++i) { QB[i] = ((c >> 2) == i) ? qv : u32x4{0u, 0u, 0u, 0u}; msk[i] = (c == 2 * i) ? 0x0000ffffu : (c == 2 * i + 1) ? 0xffff0000u : 0u; }
    }
    AttnState st; st.acc = f32x4{0.f, 0.f, 0.f, 0.f}; st.l = 0.f; st.m = -INFINITY; st.mL = -INFINITY;
    unsigned cur = 8 + 4 * (ja & 1) + wave;
#pragma unroll 1
    for (; cur < 8 * (R0 - 1); cur += 8) tile_step<true, 7>(st, K, V, QB, msk, voffK, voffV, ku + cur * 4096, vu + cur * 4096);
    {
      const unsigned char* ctr = p.ws + WS_CTR + (ja >> 1) * 256;
      unsigned cl = 0;
#pragma unroll 1
      while (cur < 512) {
        claim_issue(cl, ctr);
        tile_step<true, 8>(st, K, V, QB, msk, voffK, voffV, ku + cur * 4096, vu + cur * 4096);
        cur = claim_read<8>(cl) + 8 * R0;
      }
    }
    tile_step<false, 7>(st, K, V, QB, msk, voffK, voffV, nullptr, nullptr);
    float l = st.l; l += dppf<ROR4>(l); l += dppf<ROR8>(l); l = x32_sum(x16_sum(l));
    LAS float* part = (LAS float*)(L_PART + wave * 528);
    if (c < 8) {
#pragma unroll
      for (int r = 0; r < 4; ++r) part[4 + 8 * (4 * g + r) + c] = st.acc[r];
    }
    if (lane == 0) { part[0] = st.m; part[1] = l; }
    asm volatile("s_waitcnt lgkmcnt(0)" ::: "memory");
    __builtin_amdgcn_s_barrier();
    return;
  }

  const int w = wave - 4;
  u32x4 xv = {0u, 0u, 0u, 0u};
  asm volatile("global_load_dwordx4 %0, %1, %2" : "+v"(xv) : "v"((lane >> 5) * DM * 2 + (lane & 31) * 16), "s"((const char*)(w < 2 ? p.xA : p.xB) + ((w & 1) * 2 * DM + (j & 3) * 256) * 2) : "memory");
  if (w == 0 && lane < 16) *(LAS unsigned*)(L_CNT + 4 * lane) = 0u;
  asm volatile("s_waitcnt lgkmcnt(0)" ::: "memory");
  __builtin_amdgcn_s_barrier();
  u64 epoch;
  asm volatile("s_load_dwordx2 %0, %1, 0x0" : "=s"(epoch) : "s"(p.ws + WS_EPOCH) : "memory");
  if (w == 0 && lane == 0 && !(j & 4)) __hip_atomic_store((GAS unsigned*)(p.ws + WS_CTR + ((j & 3) * NH + (j >> 3)) * 256), 0u, __ATOMIC_RELAXED, __HIP_MEMORY_SCOPE_AGENT);
  asm volatile("s_waitcnt lgkmcnt(0)" : "+s"(epoch) :: "memory");
  Tag t;
  {
    u64 z = epoch + 0x9E3779B97F4A7C15ull;
    z = (z ^ (z >> 30)) * 0xBF58476D1CE4E5B9ull; z = (z ^ (z >> 27)) * 0x94D049BB133111EBull; z ^= z >> 31;
    t.hi = (unsigned)(z >> 32); t.f = (unsigned)z;
  }
  if (w == 3 && (j & 7) == 0) { const float v = __uint_as_float(__builtin_amdgcn_s_getreg(20 | (3 << 11)) & 7u); put2(p.ws, WS_OCT + (j >> 3) * 128 + (lane & 7) * 16, v, v, t, 0xffull); }
  waitv<0>(xv);
  *(LAS u32x4*)(L_X + w * 1024 + lane * 16) = xv;
  lds_signal(C_X);
  u32x4 F[8];
  lds_wait(C_X, 4);
  lds_wait(C_WQA, 4);
  if (w == 0) STAMP(3);
  load_wfrag(F, L_R0, w, lane);
  {
    const float v = sel4(piece(L_X, 512, lane, F), g), nb = dppf<XOR1>(v);
    put2(p.ws, WS_QA1 + j * 2048 + g * 512 + (8 * w + (c >> 1)) * 16, v, nb, t, 0x5555555555555555ull);
  }
  lds_signal(C_FREE0);
  if (w == 3) {
    u32x4 v[1]; const unsigned off[1] = {WS_OCT + (j >> 3) * 128 + (lane & 7) * 16};
    gather16<1, 4>(v, p.ws, off, t);
    const unsigned x = (v[0][0] + j) & 7;
    lds_signal(C_ROLE, 8 | ((((x & 1) ? (x ^ XPAIR) : x) & 6) | (x & 1)));
  }
  lds_wait(C_WQB, 4);
  load_wfrag(F, L_R1, w, lane);
  { const f32x4 d = piece(L_X + 2048, 512, lane, F); if (g == 0) st16<true>(p.ws, WS_QB1 + j * 1024 + (16 * w + c) * 16, asu(d)); }
  lds_signal(C_FREE1);
  const int ja = (j & ~7) | (lds_wait(C_ROLE, 8) & 7);
  if (w == 0) {
    u32x4 v[4]; unsigned off[4];
#pragma unroll
    for (int i = 0; i < 4; ++i) off[i] = WS_QA1 + (8 * ((ja >> 1) & 31) + 4 * (lane >> 5) + i) * 2048 + (ja >> 6) * 512 + (lane & 31) * 16;
    gather16<4, QA_SLEEP>(v, p.ws, off, t);
    *(LAS unsigned*)(L_QA + 4 * lane) = pack2((dec(v[0][0]) + dec(v[1][0])) + (dec(v[2][0]) + dec(v[3][0])), (dec(v[0][2]) + dec(v[1][2])) + (dec(v[2][2]) + dec(v[3][2])));
    lds_signal(C_QA);
    STAMP(10);
  }
  raise_flag(p.ws, WS_FQ + (j * 4 + w) * 8, t);
  if (w == 1 && (j & 3) == ((j >> 2) & 3)) {
    const int j0 = j & ~3;
    wait_flags(p.ws, WS_FQ + j0 * 32 + c * 8, t);
    u32x4 v[4]; fetch<true>(v, p.ws, WS_QB1 + j0 * 1024 + lane * 16, 1024);
    const f32x4 q4 = (asf(v[0]) + asf(v[1])) + (asf(v[2]) + asf(v[3]));
    const unsigned p01 = pack2(q4[0], q4[1]), p23 = pack2(q4[2], q4[3]);
    const int bb = lane & 3;
    const unsigned x0 = dppu<0x00>(p01), x1 = dppu<0x55>(p01), x2 = dppu<0xAA>(p01), x3 = dppu<0xFF>(p01);
    const unsigned y0 = dppu<0x00>(p23), y1 = dppu<0x55>(p23), y2 = dppu<0xAA>(p23), y3 = dppu<0xFF>(p23);
    const unsigned e[4] = {(bb & 2) ? y0 : x0, (bb & 2) ? y1 : x1, (bb & 2) ? y2 : x2, (bb & 2) ? y3 : x3};
    const u32x2 o = (bb & 1) ? u32x2{(e[0] >> 16) | (e[1] & 0xffff0000u), (e[2] >> 16) | (e[3] & 0xffff0000u)}
                             : u32x2{(e[0] & 0xffffu) | (e[1] << 16), (e[2] & 0xffffu) | (e[3] << 16)};
    st8(p.ws, WS_QB2 + (bb * HD + (j >> 2) * 64 + (lane & ~3)) * 2, o);
    raise_flag(p.ws, WS_FQ2 + (j >> 2) * 8, t);
  }
  if (w == 2) {
    wait_flags(p.ws, WS_FQ2 + (((j >> 4) & 3) * 16 + c) * 8, t);
    u32x4 v[2]; fetch<false>(v, p.ws, WS_QB2 + (j >> 4) * 2048 + lane * 16, 1024);
#pragma unroll
    for (int i = 0; i < 2; ++i) *(LAS u32x4*)(L_QB + (4 * i + g) * 272 + 16 * c) = v[i];
    lds_signal(C_QB);
  }
  lds_wait(C_QB, 1);
  lds_wait(C_KV, 4);
  if (w == 0) STAMP(4);
  {
    u32x4 Q[4];
#pragma unroll
    for (int kk = 0; kk < 4; ++kk) Q[kk] = lds128(L_QB + (c & 7) * 272 + 64 * kk + 16 * g);
    float s[8][4]; float mx = -INFINITY;
#pragma unroll
    for (int blk = 0; blk < 8; ++blk) {
      f32x4 d = {0.f, 0.f, 0.f, 0.f};
#pragma unroll
      for (int kk = 0; kk < 4; ++kk) d = mfma16(lds128(L_KB + (16 * blk + c) * 256 + 16 * ((4 * kk + g) ^ ((c & 3) << 2))), Q[kk], d);
#pragma unroll
      for (int r = 0; r < 4; ++r) { s[blk][r] = bf16r(bf16r(d[r]) * SCALE); mx = fmaxf(mx, s[blk][r]); }
    }
    mx = x32_max(x16_max(mx));
    const float mL = mulL(mx);
    float l = 0.f;
#pragma unroll
    for (int blk = 0; blk < 8; ++blk)
#pragma unroll
      for (int r = 0; r < 4; ++r) { s[blk][r] = ex2(__builtin_fmaf(s[blk][r], LOG2E, -mL)); l += s[blk][r]; }
    l = x32_sum(x16_sum(l));
    f32x4 o0 = {0.f, 0.f, 0.f, 0.f}, o1 = {0.f, 0.f, 0.f, 0.f};
    const int q = (lane >> 2) & 3, pp = lane & 3;
    const unsigned va0 = L_VB + (4 * g + q) * 256 + 16 * ((4 * w + (pp >> 1)) ^ (q << 2)) + 8 * (pp & 1);
#pragma unroll
    for (int tt = 0; tt < 4; ++tt) {
      const u32x4 P = {pack2(s[2 * tt][0], s[2 * tt][1]), pack2(s[2 * tt][2], s[2 * tt][3]), pack2(s[2 * tt + 1][0], s[2 * tt + 1][1]), pack2(s[2 * tt + 1][2], s[2 * tt + 1][3])};
      const u32x2 a0 = lds_tr(va0 + tt * 8192), a1 = lds_tr(va0 + tt * 8192 + 4096), b0 = lds_tr(va0 + tt * 8192 + 32), b1 = lds_tr(va0 + tt * 8192 + 4096 + 32);
      o0 = mfma16(u32x4{a0[0], a0[1], a1[0], a1[1]}, P, o0);
      o1 = mfma16(u32x4{b0[0], b0[1], b1[0], b1[1]}, P, o1);
    }
    const int rec = ((j >> 4) * 8 + (c & 7)) * 16 + (j & 15);
    st16<true>(p.ws, WS_PB + rec * 512 + (32 * w + 16 * (c >> 3) + 4 * g) * 4, asu(c < 8 ? o0 : o1));
    if (w == 0 && lane < 8) st8(p.ws, WS_PBML + rec * 8, u32x2{__float_as_uint(mL), __float_as_uint(l)});
    raise_flag(p.ws, WS_FP + (j * 4 + w) * 8, t);
  }
  if (w == 3) {
    wait_flags(p.ws, WS_FP + (j >> 4) * 512 + lane * 8, t);
    u32x2 ml = {0u, 0u};
    ld8(ml, p.ws, WS_PBML + ((j >> 1) * 16 + 4 * (lane & 3) + g) * 8);
    u32x4 v[4]; fetch<true>(v, p.ws, WS_PB + ((j >> 1) * 16 + g) * 512 + (j & 1) * 256 + c * 16, 2048);
    pin(ml);
    const float m = __uint_as_float(ml[0]);
    float M = fmaxf(m, dppf<XOR1>(m)); M = fmaxf(M, dppf<XOR2>(M)); M = x32_max(x16_max(M));
    const float wt = ex2(m - M);
    float L = wt * __uint_as_float(ml[1]); L += dppf<XOR1>(L); L += dppf<XOR2>(L); L = x32_sum(x16_sum(L));
    f32x4 a = asf(v[0]) * dppf<0x00>(wt) + asf(v[1]) * dppf<0x55>(wt) + asf(v[2]) * dppf<0xAA>(wt) + asf(v[3]) * dppf<0xFF>(wt);
#pragma unroll
    for (int r = 0; r < 4; ++r) a[r] = x32_sum(x16_sum(a[r])) / L;
    const int slot = 8 * ((j >> 1) & 31) + 2 * (j >> 6) + (j & 1);
    if (lane < 16) st8(p.ws, WS_OB + slot * 128 + c * 8, u32x2{pack2(a[0], a[1]), pack2(a[2], a[3])});
    raise_flag(p.ws, WS_FO + slot * 8, t);
  }
  if (w == 2) {
    wait_flags(p.ws, WS_FO + ((j & ~15) + c) * 8, t);
    u32x4 v[2]; fetch<false>(v, p.ws, WS_OB + (j >> 4) * 2048 + lane * 16, 1024);
#pragma unroll
    for (int i = 0; i < 2; ++i) *(LAS u32x4*)(L_OB + g * 576 + 256 * i + 16 * c) = v[i];
    lds_signal(C_OB);
  }
  lds_wait(C_OB, 1);
  lds_wait(C_WOB, 4);
  {
    load_wfrag(F, L_R1, w, lane);
    const f32x4 d = piece(L_OB, 576, lane, F);
    const int cb16 = 4 * (j & 15) + w;
    if (g == 0) st16<true>(p.ws, WS_YB + cb16 * 4096 + (j >> 4) * 256 + c * 16, asu(d));
    raise_flag(p.ws, WS_FY + cb16 * 128 + (j >> 4) * 8, t);
  }
  if (w == 1 && (j & 3) == ((j >> 2) & 3)) {
    wait_flags(p.ws, WS_FY + (j >> 2) * 128 + c * 8, t);
    u32x4 v[4]; fetch<true>(v, p.ws, WS_YB + (j >> 2) * 4096 + lane * 16, 1024);
    f32x4 a = (asf(v[0]) + asf(v[1])) + (asf(v[2]) + asf(v[3]));
#pragma unroll
    for (int r = 0; r < 4; ++r) a[r] = x32_sum(x16_sum(a[r]));
    p.out[g * 2 * DM + DM + 16 * (j >> 2) + c] = f2bf(sel4(a, g));
  }
  if (w == 1) STAMP(5);
  load_wfrag(F, L_R0, w, lane);
#pragma unroll
  for (int kk = 0; kk < 8; ++kk) pin(F[kk]);
  waitvm<0>();
  u64 on = DRY ? 0ull : ~0ull;
#pragma unroll 1
  while (true) {
    asm volatile("" : "+s"(on));
    const bool live = on != 0;
    asm volatile("s_waitcnt lgkmcnt(0)" ::: "memory");
    if (live) __builtin_amdgcn_s_barrier();
    asm volatile("" ::: "memory");
    if (w == 0) {
      STAMP(6);
      f32x2 ml[4], oo[4];
#pragma unroll
      for (int i = 0; i < 4; ++i) { ml[i] = lds64f(L_PART + i * 528); oo[i] = lds64f(L_PART + i * 528 + 16 + 8 * lane); }
      const float M = fmaxf(fmaxf(ml[0][0], ml[1][0]), fmaxf(ml[2][0], ml[3][0])), ML = mulL(M);
      float L = 0.f, a0 = 0.f, a1 = 0.f;
#pragma unroll
      for (int i = 0; i < 4; ++i) { const float wt = ex2(mulL(ml[i][0]) - ML); L = __builtin_fmaf(wt, ml[i][1], L); a0 = __builtin_fmaf(wt, oo[i][0], a0); a1 = __builtin_fmaf(wt, oo[i][1], a1); }
      const unsigned rec = WS_PA + (8 * ((ja >> 1) & 31) + 2 * (ja >> 6) + (ja & 1)) * PA_REC;
      put2(p.ws, rec + lane * 16, a0, a1, t, on);
      put2(p.ws, rec + 1024 + (lane & 7) * 16, M, L, t, on & 0xffull);
    }
    {
      u32x4 v[8]; unsigned off[8];
#pragma unroll
      for (int i = 0; i < 4; ++i) { const unsigned rec = WS_PA + ((j & ~15) + 8 * (i >> 1) + 2 * w + (i & 1)) * PA_REC; off[i] = rec + lane * 16; off[4 + i] = rec + 1024 + (lane & 7) * 16; }
      gather16<8, PA_SLEEP>(v, p.ws, off, t, on);
      if (w == 0) STAMP(7);
#pragma unroll
      for (int hh = 0; hh < 2; ++hh) {
        const float m0 = dec(v[4 + 2 * hh][0]), m1 = dec(v[5 + 2 * hh][0]), ML = mulL(fmaxf(m0, m1));
        const float w0 = ex2(mulL(m0) - ML), w1 = ex2(mulL(m1) - ML);
        const float L = w0 * dec(v[4 + 2 * hh][2]) + w1 * dec(v[5 + 2 * hh][2]);
        const float a0 = w0 * dec(v[2 * hh][0]) + w1 * dec(v[2 * hh + 1][0]), a1 = w0 * dec(v[2 * hh][2]) + w1 * dec(v[2 * hh + 1][2]);
        const float rl = __builtin_amdgcn_rcpf(L);
        *(LAS unsigned*)(L_OA + w * 576 + 256 * hh + 4 * lane) = pack2(a0 * rl, a1 * rl);
      }
      asm volatile("s_waitcnt lgkmcnt(0)" ::: "memory");
      if (live) __builtin_amdgcn_s_barrier();
      asm volatile("" ::: "memory");
      const f32x4 d = piece(L_OA, 576, lane, F);
      const int n = 64 * (j & 15) + 16 * w + c;
      put2(p.ws, WS_YA + ((g & 1) * 128 + (n >> 3)) * 2048 + (j >> 4) * 128 + (n & 7) * 16, g ? d[2] : d[0], g ? d[3] : d[1], t, on & 0xffffffffull);
    }
    if (w == 0) {
      STAMP(8);
      u32x4 v[2]; const unsigned off[2] = {WS_YA + j * 2048 + lane * 16, WS_YA + j * 2048 + 1024 + lane * 16};
      gather16<2, Y_SLEEP>(v, p.ws, off, t, on);
      float a = dec(v[0][0]) + dec(v[1][0]), b = dec(v[0][2]) + dec(v[1][2]);
      a += dppf<ROR8>(a); b += dppf<ROR8>(b);
      a = x32_sum(x16_sum(a)); b = x32_sum(x16_sum(b));
      u64 keep;
      asm volatile("s_mov_b64 %0, exec\n\ts_mov_b64 exec, %4\n\tglobal_store_short %1, %2, %3\n\ts_mov_b64 exec, %0"
                   : "=&s"(keep) : "v"(((2 * (j >> 7) + ((lane >> 3) & 1)) * 2 * DM + 8 * (j & 127) + (lane & 7)) * 2), "v"((unsigned)f2bf(lane < 8 ? a : b)), "s"(p.out), "s"(on & 0xffffull) : "memory");
      STAMP(9);
    }
    if (live) break;
    on = ~0ull;
  }
  if (w == 0) {
    if (j == 0 && lane == 0) __hip_atomic_store((GAS u64*)(p.ws + WS_EPOCH), epoch + 1, __ATOMIC_RELAXED, __HIP_MEMORY_SCOPE_AGENT);
  }
}

extern "C" void kernel_launch(void* const* d_in, const int* in_sizes, int n_in, void* d_out, int out_size, void* d_ws, size_t ws_size, hipStream_t stream) {
  Params p;
  p.xA = (const u16*)d_in[0]; p.kA = (const u16*)d_in[1]; p.vA = (const u16*)d_in[2]; p.wqA = (const u16*)d_in[3]; p.woA = (const u16*)d_in[4];
  p.xB = (const u16*)d_in[5]; p.kB = (const u16*)d_in[6]; p.vB = (const u16*)d_in[7]; p.wqB = (const u16*)d_in[8]; p.woB = (const u16*)d_in[9];
  p.out = (u16*)d_out; p.ws = (unsigned char*)d_ws;
  (void)hipFuncSetAttribute((const void*)fused_decode, hipFuncAttributeMaxDynamicSharedMemorySize, LDS_BYTES);
  fused_decode<<<NWG, NTHR, LDS_BYTES, stream>>>(p);
}
```

```cpp
#include <hip/hip_runtime.h>
#include <hip/hip_bf16.h>
#include <cstdint>
#pragma clang diagnostic ignored "-Wint-to-pointer-cast"

typedef __attribute__((ext_vector_type(4))) unsigned u32x4;
typedef __attribute__((ext_vector_type(2))) unsigned u32x2;
typedef __attribute__((ext_vector_type(4))) float f32x4;
typedef __attribute__((ext_vector_type(2))) float f32x2;
typedef __attribute__((ext_vector_type(8))) __bf16 bf16x8;
typedef __attribute__((ext_vector_type(2))) __bf16 bf16x2;
typedef __attribute__((ext_vector_type(4))) short s16x4;
typedef unsigned short u16;
typedef unsigned long long u64;
#define LAS __attribute__((address_space(3)))
#define GAS __attribute__((address_space(1)))
#define INL __device__ __forceinline__

constexpr int DM = 1024, NH = 32, DH = 128, SK = 8192, WIN = 2048, HD = NH * DH;
constexpr float SCALE = 0.088388347648318440f, LOG2E = 1.4426950408889634f;
constexpr int NWG = 256, NTHR = 512;

#define R0 56
#define PM 17
#define QA_SLEEP 8
#define B_SLEEP 64
#define PA_SLEEP 2
#define Y_SLEEP 1
#define DRY 0
#define LATCH 1
#define XPAIR 3

constexpr unsigned WS_EPOCH = 0;
constexpr unsigned WS_OCT = 4096;
constexpr unsigned WS_QA1 = WS_OCT + 32 * 128;
constexpr unsigned PA_REC = 1152;
constexpr unsigned WS_PA = WS_QA1 + NWG * 2048;
constexpr unsigned WS_YA = WS_PA + NWG * PA_REC;
constexpr unsigned WS_CTR = WS_YA + NWG * 2048;
constexpr unsigned WS_QB1 = WS_CTR + 128 * 256;
constexpr unsigned WS_QB2 = WS_QB1 + NWG * 1024;
constexpr unsigned WS_PB = WS_QB2 + 128 * 256;
constexpr unsigned WS_PBML = WS_PB + 128 * 16 * 512;
constexpr unsigned WS_OB = WS_PBML + 128 * 16 * 8;
constexpr unsigned WS_YB = WS_OB + 128 * 256;
constexpr unsigned WS_FQ = WS_YB + 64 * 4096;
constexpr unsigned WS_FQ2 = WS_FQ + NWG * 32;
constexpr unsigned WS_FP = WS_FQ2 + 64 * 8;
constexpr unsigned WS_FO = WS_FP + NWG * 32;
constexpr unsigned WS_FY = WS_FO + NWG * 8;
constexpr unsigned WS_END = WS_FY + 64 * 128;
static_assert(WS_END <= (4u << 20), "workspace");

constexpr unsigned L_R0 = 0, L_R1 = 32768, L_KB = 65536, L_VB = 98304;
constexpr unsigned L_QA = 131072;
constexpr unsigned L_QB = L_QA + 256;
constexpr unsigned L_OA = L_QB + 8 * 272;
constexpr unsigned L_OB = L_OA + 4 * 576;
constexpr unsigned L_PART = L_OB + 4 * 576;
constexpr unsigned L_X = L_PART + 4 * 528;
constexpr unsigned L_CNT = L_X + 4096;
constexpr unsigned LDS_BYTES = L_CNT + 64 + 256;
enum { C_WQA = 0, C_WQB, C_KV, C_FREE0, C_FREE1, C_QA, C_WOB, C_QB, C_OB, C_X, C_ROLE };
constexpr unsigned SPIN_CAP = 1u << 17;

#define STAMP(i) do { } while (0)

struct Params {
  const u16 *xA, *kA, *vA, *wqA, *woA, *xB, *kB, *vB, *wqB, *woB;
  u16* out;
  unsigned char* ws;
};

template <int V> struct IC { static constexpr int value = V; };
template <int A, int B, typename F> INL void static_for(F&& f) { if constexpr (A < B) { f(IC<A>{}); static_for<A + 1, B>(f); } }
INL float bf2f(u16 v) { return __uint_as_float(((unsigned)v) << 16); }
INL u16 f2bf(float f) { return __builtin_bit_cast(u16, (__bf16)f); }
INL float bf16r(float f) { return bf2f(f2bf(f)); }
INL unsigned pack2(float lo, float hi) { return __builtin_bit_cast(unsigned, __builtin_convertvector(f32x2{lo, hi}, bf16x2)); }
template <int CTRL> INL float dppf(float x) { return __builtin_bit_cast(float, __builtin_amdgcn_mov_dpp(__builtin_bit_cast(int, x), CTRL, 0xf, 0xf, true)); }
template <int CTRL> INL unsigned dppu(unsigned x) { return (unsigned)__builtin_amdgcn_mov_dpp((int)x, CTRL, 0xf, 0xf, true); }
constexpr int XOR1 = 0xB1, XOR2 = 0x4E, ROR4 = 0x124, ROR8 = 0x128, BCAST = 0x150;
INL float x16_max(float x) { auto s = __builtin_amdgcn_permlane16_swap(__float_as_uint(x), __float_as_uint(x), false, false); return fmaxf(__uint_as_float(s[0]), __uint_as_float(s[1])); }
INL float x32_max(float x) { auto s = __builtin_amdgcn_permlane32_swap(__float_as_uint(x), __float_as_uint(x), false, false); return fmaxf(__uint_as_float(s[0]), __uint_as_float(s[1])); }
INL float x16_sum(float x) { auto s = __builtin_amdgcn_permlane16_swap(__float_as_uint(x), __float_as_uint(x), false, false); return __uint_as_float(s[0]) + __uint_as_float(s[1]); }
INL float x32_sum(float x) { auto s = __builtin_amdgcn_permlane32_swap(__float_as_uint(x), __float_as_uint(x), false, false); return __uint_as_float(s[0]) + __uint_as_float(s[1]); }
INL f32x4 mfma16(u32x4 a, u32x4 b, f32x4 c) { return __builtin_amdgcn_mfma_f32_16x16x32_bf16(__builtin_bit_cast(bf16x8, a), __builtin_bit_cast(bf16x8, b), c, 0, 0, 0); }
INL float ex2(float x) { return __builtin_amdgcn_exp2f(x); }
INL float mulL(float m) { float r = m * LOG2E; asm volatile("" : "+v"(r)); return r; }
INL u32x4 asu(const f32x4& v) { return __builtin_bit_cast(u32x4, v); }
INL f32x4 asf(const u32x4& v) { return __builtin_bit_cast(f32x4, v); }
INL float sel4(const f32x4& d, int g) { return (g & 2) ? ((g & 1) ? d[3] : d[2]) : ((g & 1) ? d[1] : d[0]); }
template <typename T> INL void pin(T& d) { asm volatile("" : "+v"(d)); }

INL u32x4 lds128(unsigned a) { return *(const LAS u32x4*)a; }
INL f32x2 lds64f(unsigned a) { return *(const LAS f32x2*)a; }
INL u32x2 lds_tr(unsigned a) { return __builtin_bit_cast(u32x2, __builtin_amdgcn_ds_read_tr16_b64_v4i16((LAS s16x4*)a)); }
INL void lds_signal(int c, unsigned n = 1) {
  u64 keep;
  asm volatile("s_waitcnt lgkmcnt(0)\n\ts_mov_b64 %0, exec\n\ts_mov_b64 exec, 1\n\tds_add_u32 %1, %2\n\ts_mov_b64 exec, %0" : "=&s"(keep) : "v"(L_CNT + 4 * c), "v"(n) : "memory");
}
INL unsigned lds_wait(int c, unsigned n) {
  unsigned v = 0;
#pragma unroll 1
  for (unsigned s = 0; s < SPIN_CAP * 8; ++s) {
    v = __builtin_amdgcn_readfirstlane(*(volatile LAS unsigned*)(L_CNT + 4 * c));
    if (v >= n) break;
    __builtin_amdgcn_s_sleep(1);
  }
  asm volatile("" ::: "memory");
  return v;
}

template <int OFF, bool NT = true> INL void aload(u32x4& d, unsigned voff, const void* sbase) {
  if (NT) asm volatile("global_load_dwordx4 %0, %1, %2 offset:%3 nt" : "+v"(d) : "v"(voff), "s"(sbase), "i"(OFF) : "memory");
  else asm volatile("global_load_dwordx4 %0, %1, %2 offset:%3" : "+v"(d) : "v"(voff), "s"(sbase), "i"(OFF) : "memory");
}
template <int N> INL void waitv(u32x4& d) { asm volatile("s_waitcnt vmcnt(%1)" : "+v"(d) : "i"(N) : "memory"); }
template <int N> INL void waitvm() { asm volatile("s_waitcnt vmcnt(%0)" :: "i"(N) : "memory"); }
INL void claim_issue(unsigned& cl, const unsigned char* ctr) {
  u64 keep;
  asm volatile("s_mov_b64 %0, exec\n\ts_mov_b64 exec, 1\n\tglobal_atomic_add %1, %2, %3, %4 sc0 sc1\n\ts_mov_b64 exec, %0"
               : "=&s"(keep), "+v"(cl) : "v"(0u), "v"(1u), "s"(ctr) : "memory");
}
template <int W> INL unsigned claim_read(unsigned& cl) {
  unsigned got;
  asm volatile("s_waitcnt vmcnt(%2)\n\tv_readfirstlane_b32 %0, %1" : "=s"(got), "+v"(cl) : "i"(W) : "memory");
  return got;
}

template <bool NT> INL void st16(const unsigned char* ws, unsigned off, const u32x4& v) {
  if (NT) asm volatile("global_store_dwordx4 %0, %1, %2 sc1 nt\n\ts_nop 1" :: "v"(off), "v"(v), "s"(ws) : "memory");
  else asm volatile("global_store_dwordx4 %0, %1, %2 sc1\n\ts_nop 1" :: "v"(off), "v"(v), "s"(ws) : "memory");
}
INL void st8(const unsigned char* ws, unsigned off, const u32x2& v) { asm volatile("global_store_dwordx2 %0, %1, %2 sc1" :: "v"(off), "v"(v), "s"(ws) : "memory"); }
template <bool NT> INL void ld16(u32x4& d, const unsigned char* ws, unsigned off) {
  if (NT) asm volatile("global_load_dwordx4 %0, %1, %2 sc1 nt" : "+v"(d) : "v"(off), "s"(ws) : "memory");
  else asm volatile("global_load_dwordx4 %0, %1, %2 sc1" : "+v"(d) : "v"(off), "s"(ws) : "memory");
}
INL void ld8(u32x2& d, const unsigned char* ws, unsigned off) { asm volatile("global_load_dwordx2 %0, %1, %2 sc1" : "+v"(d) : "v"(off), "s"(ws) : "memory"); }
template <bool NT, int N> INL void fetch(u32x4 (&v)[N], const unsigned char* ws, unsigned off, unsigned stride) {
#pragma unroll
  for (int i = 0; i < N; ++i) { v[i] = u32x4{0u, 0u, 0u, 0u}; ld16<NT>(v[i], ws, off + i * stride); }
  waitvm<0>();
#pragma unroll
  for (int i = 0; i < N; ++i) pin(v[i]);
}

struct Tag { unsigned hi, f; };
INL float dec(unsigned lo) { return __uint_as_float(lo); }
INL void put2(const unsigned char* ws, unsigned off, float a, float b, const Tag& t, u64 mask) {
  u64 keep;
  asm volatile("s_mov_b64 %0, exec\n\ts_mov_b64 exec, %4\n\tglobal_store_dwordx4 %1, %2, %3 sc1\n\ts_mov_b64 exec, %0\n\ts_nop 0"
               : "=&s"(keep) : "v"(off), "v"(u32x4{__float_as_uint(a), t.hi, __float_as_uint(b), t.hi}), "s"(ws), "s"(mask) : "memory");
}
INL void ld16m(u32x4& d, const unsigned char* ws, unsigned off, u64 mask) {
  asm volatile("s_mov_b64 exec, %3\n\tglobal_load_dwordx4 %0, %1, %2 sc1\n\ts_mov_b64 exec, -1" : "+v"(d) : "v"(off), "s"(ws), "s"(mask) : "memory");
}
template <int N, int SLEEP> INL void gather16(u32x4 (&v)[N], const unsigned char* ws, const unsigned (&off)[N], const Tag& t, u64 on = ~0ull) {
  u64 need[N];
#pragma unroll
  for (int i = 0; i < N; ++i) { asm volatile("" : "=v"(v[i])); need[i] = on; }
#pragma unroll 1
  for (unsigned s = 0; s < SPIN_CAP; ++s) {
#pragma unroll
    for (int i = 0; i < N; ++i) ld16m(v[i], ws, off[i], need[i]);
    waitvm<0>();
    unsigned bad[N], any = 0;
#pragma unroll
    for (int i = 0; i < N; ++i) { pin(v[i]); bad[i] = (v[i][1] ^ t.hi) | (v[i][3] ^ t.hi); pin(bad[i]); any |= bad[i]; }
    if ((__builtin_amdgcn_ballot_w64(any != 0) & on) == 0) break;
#pragma unroll
    for (int i = 0; i < N; ++i) {
      const u64 m = __builtin_amdgcn_ballot_w64(bad[i] != 0);
      asm volatile("s_cmp_lg_u64 %1, 0\n\ts_cselect_b64 %0, -1, 0" : "=s"(need[i]) : "s"(m) : "scc");
    }
    if (SLEEP) __builtin_amdgcn_s_sleep(SLEEP);
  }
}
INL void raise_flag(const unsigned char* ws, unsigned off, const Tag& t) {
  waitvm<0>();
  if ((threadIdx.x & 63) == 0) st8(ws, off, u32x2{t.f, t.hi});
}
INL void wait_flags(const unsigned char* ws, unsigned off, const Tag& t) {
  u32x2 v = {0u, 0u};
#pragma unroll 1
  for (unsigned s = 0; s < SPIN_CAP; ++s) {
    __builtin_amdgcn_s_sleep(B_SLEEP);
    ld8(v, ws, off); waitvm<0>(); pin(v);
    if (__builtin_amdgcn_ballot_w64(((v[0] ^ t.f) | (v[1] ^ t.hi)) != 0) == 0) break;
  }
}

struct AttnState { f32x4 acc; float l, m, mL; };
template <bool RELOAD, int W>
INL void tile_step(AttnState& st, u32x4 (&K)[4], u32x4 (&V)[4], const u32x4 (&QB)[4], const unsigned (&msk)[4],
                   unsigned voffK, unsigned voffV, const char* kn, const char* vn) {
  f32x4 s = {0.f, 0.f, 0.f, 0.f};
#define QKI(i) waitv<RELOAD ? W : W - i>(K[i]); s = mfma16(K[i], QB[i], s); if (RELOAD) { __builtin_amdgcn_sched_barrier(0); aload<1024 * i>(K[i], voffK, kn); }
  QKI(0) QKI(1) QKI(2) QKI(3)
#undef QKI
  float d = dppf<0x00>(s[0]); d += dppf<0x55>(s[1]); d += dppf<0xAA>(s[2]); d += dppf<0xFF>(s[3]);
  const float s2 = bf16r(bf16r(d) * SCALE);
  if (__builtin_amdgcn_ballot_w64(s2 > st.m) != 0) {
    float mx = fmaxf(s2, dppf<ROR4>(s2)); mx = fmaxf(mx, dppf<ROR8>(mx)); mx = x32_max(x16_max(mx));
    mx = fmaxf(mx, st.m);
    const float mxL = mulL(mx), al = ex2(st.mL - mxL);
    st.acc *= al; st.l *= al; st.m = mx; st.mL = mxL;
  }
  const float pr = ex2(__builtin_fmaf(s2, LOG2E, -st.mL));
  st.l += pr;
  const unsigned pk = pack2(pr, pr);
#define PVI(i) { u32x4 B; B[0] = dppu<BCAST + 4 * i>(pk) & msk[0]; B[1] = dppu<BCAST + 4 * i>(pk) & msk[1]; B[2] = dppu<BCAST + 4 * i>(pk) & msk[2]; B[3] = dppu<BCAST + 4 * i>(pk) & msk[3]; \
    waitv<RELOAD ? W : W - 4 - i>(V[i]); st.acc = mfma16(V[i], B, st.acc); if (RELOAD) { __builtin_amdgcn_sched_barrier(0); aload<1024 * i>(V[i], voffV, vn); } }
  PVI(0) PVI(1) PVI(2) PVI(3)
#undef PVI
}

INL void load_wfrag(u32x4 (&B)[8], unsigned region, int w, int lane) {
  const int g = lane >> 4, q = (lane >> 2) & 3, pp = lane & 3;
  const unsigned sw = 2u * ((q >> 1) | ((g & 1) << 1));
  const unsigned a = region + (8 * g + q) * 128 + 16 * ((2 * w + (pp >> 1)) ^ sw) + 8 * (pp & 1);
#pragma unroll
  for (int kk = 0; kk < 8; ++kk) { const u32x2 lo = lds_tr(a + kk * 4096), hi = lds_tr(a + kk * 4096 + 512); B[kk] = u32x4{lo[0], lo[1], hi[0], hi[1]}; }
}
INL f32x4 piece(unsigned vec, unsigned vstride, int lane, const u32x4 (&B)[8]) {
  const unsigned a = vec + (lane & 3) * vstride + 16 * (lane >> 4);
  u32x4 A[8];
#pragma unroll
  for (int kk = 0; kk < 8; ++kk) A[kk] = lds128(a + 64 * kk);
  __builtin_amdgcn_sched_barrier(0);
  f32x4 d0 = {0.f, 0.f, 0.f, 0.f}, d1 = {0.f, 0.f, 0.f, 0.f};
#pragma unroll
  for (int kk = 0; kk < 8; kk += 2) { d0 = mfma16(A[kk], B[kk], d0); d1 = mfma16(A[kk + 1], B[kk + 1], d1); }
  return d0 + d1;
}
INL unsigned wtile_voff(int lane, int odd, unsigned rs) {
  const unsigned sw = 2u * (((lane >> 4) & 1) | (odd << 1));
  return (lane >> 3) * rs + 16 * ((lane & 7) ^ sw);
}

__global__ void __launch_bounds__(NTHR, 2) fused_decode(Params p) {
  const int lane = threadIdx.x & 63, wave = __builtin_amdgcn_readfirstlane(threadIdx.x >> 6);
  const int j = blockIdx.x, g = lane >> 4, c = lane & 15;

  if (wave < 4) {
    if (wave == 0) STAMP(0);
    const unsigned vq0 = wtile_voff(lane, 0, HD * 2), vq1 = wtile_voff(lane, 1, HD * 2);
    const unsigned vo0 = wtile_voff(lane, 0, DM * 2), vo1 = wtile_voff(lane, 1, DM * 2);
    const unsigned vkv = g * 256 + 16 * (c ^ (g << 2));
    const size_t qoff = ((size_t)((j & 3) * 256 + wave * 64) * HD + (j >> 2) * 64) * 2;
    const size_t ooff = ((size_t)((j >> 4) * 256 + wave * 64) * DM + (j & 15) * 64) * 2;
    const size_t boff = ((size_t)(j >> 4) * SK + (SK - WIN) + (j & 15) * 128 + wave * 32) * DH * 2;
    const unsigned voffV = lane * 16, voffK = ((lane >> 2) & 3) * 256 + g * 64 + (lane & 3) * 16;
    const unsigned ldst = wave * 8192 + lane * 16;
    u32x4 K[4], V[4];
#pragma unroll
    for (int i = 0; i < 4; ++i) { K[i] = u32x4{0u, 0u, 0u, 0u}; V[i] = u32x4{0u, 0u, 0u, 0u}; }
    int ja = 0; const char *ku = nullptr, *vu = nullptr;
    static_for<0, 56>([&](auto nc) __attribute__((always_inline)) {
      constexpr int n = decltype(nc)::value, k = n >> 3, i = n & 7;
      u32x4& R = i < 4 ? K[i & 3] : V[i & 3];
      if constexpr (n == 8) __builtin_amdgcn_s_barrier();
      if constexpr (n == 40) lds_wait(C_FREE0, 4);
      if constexpr (n == 48) {
        lds_wait(C_FREE1, 4);
        ja = (j & ~7) | (lds_wait(C_ROLE, 8) & 7);
        ku = (const char*)p.kA + (size_t)(ja >> 1) * SK * DH * 2; vu = (const char*)p.vA + (size_t)(ja >> 1) * SK * DH * 2;
      }
      if constexpr (n >= 8) {
        constexpr unsigned reg = k == 1 ? L_R0 : k == 2 ? L_R1 : k == 3 ? L_KB : k == 4 ? L_VB : k == 5 ? L_R0 : L_R1;
        asm volatile("s_waitcnt vmcnt(7)\n\tds_write_b128 %1, %0 offset:%2" : "+v"(R) : "v"(ldst + (reg & ~0xffffu)), "i"((reg & 0xffffu) + i * 1024) : "memory");
      }
      if constexpr (k == 0) aload<0, !((PM >> 0) & 1)>(R, (i & 1) ? vq1 : vq0, (const char*)p.wqA + qoff + (size_t)i * 8 * HD * 2);
      if constexpr (k == 1) aload<0, !((PM >> 1) & 1)>(R, (i & 1) ? vq1 : vq0, (const char*)p.wqB + qoff + (size_t)i * 8 * HD * 2);
      if constexpr (k == 2) aload<(i & 3) * 1024, !((PM >> 2) & 1)>(R, vkv, (const char*)p.kB + boff + (i >> 2) * 4096);
      if constexpr (k == 3) aload<(i & 3) * 1024, !((PM >> 3) & 1)>(R, vkv, (const char*)p.vB + boff + (i >> 2) * 4096);
      if constexpr (k == 4) aload<0, !((PM >> 4) & 1)>(R, (i & 1) ? vo1 : vo0, (const char*)p.woA + ooff + (size_t)i * 8 * DM * 2);
      if constexpr (k == 5) aload<0, !((PM >> 5) & 1)>(R, (i & 1) ? vo1 : vo0, (const char*)p.woB + ooff + (size_t)i * 8 * DM * 2);
      if constexpr (k == 6) aload<(i & 3) * 1024>(R, i < 4 ? voffK : voffV, (i < 4 ? ku : vu) + (4 * (ja & 1) + wave) * 4096);
      if constexpr (n == 15) lds_signal(C_WQA);
      if constexpr (n == 23) lds_signal(C_WQB);
      if constexpr (n == 39) lds_signal(C_KV);
      if constexpr (n == 55) lds_signal(C_WOB);
    });
    if (wave == 0) STAMP(1);
    lds_wait(C_QA, 1);
    if (wave == 0) STAMP(2);
    u32x4 QB[4]; unsigned msk[4];
    {
      const u32x4 qv = lds128(L_QA + 16 * (4 * g + (lane & 3)));
#pragma unroll
      for (int i = 0; i < 4; ++i) { QB[i] = ((c >> 2) == i) ? qv : u32x4{0u, 0u, 0u, 0u}; msk[i] = (c == 2 * i) ? 0x0000ffffu : (c == 2 * i + 1) ? 0xffff0000u : 0u; }
    }
    AttnState st; st.acc = f32x4{0.f, 0.f, 0.f, 0.f}; st.l = 0.f; st.m = -INFINITY; st.mL = -INFINITY;
    unsigned cur = 8 + 4 * (ja & 1) + wave;
#pragma unroll 1
    for (; cur < 8 * (R0 - 1); cur += 8) tile_step<true, 7>(st, K, V, QB, msk, voffK, voffV, ku + cur * 4096, vu + cur * 4096);
    {
      const unsigned char* ctr = p.ws + WS_CTR + (ja >> 1) * 256;
      unsigned cl = 0;
#pragma unroll 1
      while (cur < 512) {
        claim_issue(cl, ctr);
        tile_step<true, 8>(st, K, V, QB, msk, voffK, voffV, ku + cur * 4096, vu + cur * 4096);
        cur = claim_read<8>(cl) + 8 * R0;
      }
    }
    tile_step<false, 7>(st, K, V, QB, msk, voffK, voffV, nullptr, nullptr);
    float l = st.l; l += dppf<ROR4>(l); l += dppf<ROR8>(l); l = x32_sum(x16_sum(l));
    LAS float* part = (LAS float*)(L_PART + wave * 528);
    if (c < 8) {
#pragma unroll
      for (int r = 0; r < 4; ++r) part[4 + 8 * (4 * g + r) + c] = st.acc[r];
    }
    if (lane == 0) { part[0] = st.m; part[1] = l; }
    asm volatile("s_waitcnt lgkmcnt(0)" ::: "memory");
    __builtin_amdgcn_s_barrier();
    return;
  }

  const int w = wave - 4;
  u32x4 xv = {0u, 0u, 0u, 0u};
  asm volatile("global_load_dwordx4 %0, %1, %2" : "+v"(xv) : "v"((lane >> 5) * DM * 2 + (lane & 31) * 16), "s"((const char*)(w < 2 ? p.xA : p.xB) + ((w & 1) * 2 * DM + (j & 3) * 256) * 2) : "memory");
  if (w == 0 && lane < 16) *(LAS unsigned*)(L_CNT + 4 * lane) = 0u;
  asm volatile("s_waitcnt lgkmcnt(0)" ::: "memory");
  __builtin_amdgcn_s_barrier();
  u64 epoch;
  asm volatile("s_load_dwordx2 %0, %1, 0x0" : "=s"(epoch) : "s"(p.ws + WS_EPOCH) : "memory");
  if (w == 0 && lane == 0 && !(j & 4)) __hip_atomic_store((GAS unsigned*)(p.ws + WS_CTR + ((j & 3) * NH + (j >> 3)) * 256), 0u, __ATOMIC_RELAXED, __HIP_MEMORY_SCOPE_AGENT);
  asm volatile("s_waitcnt lgkmcnt(0)" : "+s"(epoch) :: "memory");
  Tag t;
  {
    u64 z = epoch + 0x9E3779B97F4A7C15ull;
    z = (z ^ (z >> 30)) * 0xBF58476D1CE4E5B9ull; z = (z ^ (z >> 27)) * 0x94D049BB133111EBull; z ^= z >> 31;
    t.hi = (unsigned)(z >> 32); t.f = (unsigned)z;
  }
  if (w == 3 && (j & 7) == 0) { const float v = __uint_as_float(__builtin_amdgcn_s_getreg(20 | (3 << 11)) & 7u); put2(p.ws, WS_OCT + (j >> 3) * 128 + (lane & 7) * 16, v, v, t, 0xffull); }
  waitv<0>(xv);
  *(LAS u32x4*)(L_X + w * 1024 + lane * 16) = xv;
  lds_signal(C_X);
  u32x4 F[8];
  lds_wait(C_X, 4);
  lds_wait(C_WQA, 4);
  if (w == 0) STAMP(3);
  load_wfrag(F, L_R0, w, lane);
  {
    const float v = sel4(piece(L_X, 512, lane, F), g), nb = dppf<XOR1>(v);
    put2(p.ws, WS_QA1 + j * 2048 + g * 512 + (8 * w + (c >> 1)) * 16, v, nb, t, 0x5555555555555555ull);
  }
  lds_signal(C_FREE0);
  if (w == 3) {
    u32x4 v[1]; const unsigned off[1] = {WS_OCT + (j >> 3) * 128 + (lane & 7) * 16};
    gather16<1, 4>(v, p.ws, off, t);
    const unsigned x = (v[0][0] + j) & 7;
    lds_signal(C_ROLE, 8 | ((((x & 1) ? (x ^ XPAIR) : x) & 6) | (x & 1)));
  }
  lds_wait(C_WQB, 4);
  load_wfrag(F, L_R1, w, lane);
  { const f32x4 d = piece(L_X + 2048, 512, lane, F); if (g == 0) st16<true>(p.ws, WS_QB1 + j * 1024 + (16 * w + c) * 16, asu(d)); }
  lds_signal(C_FREE1);
  const int ja = (j & ~7) | (lds_wait(C_ROLE, 8) & 7);
  if (w == 0) {
    u32x4 v[4]; unsigned off[4];
#pragma unroll
    for (int i = 0; i < 4; ++i) off[i] = WS_QA1 + (8 * ((ja >> 1) & 31) + 4 * (lane >> 5) + i) * 2048 + (ja >> 6) * 512 + (lane & 31) * 16;
    gather16<4, QA_SLEEP>(v, p.ws, off, t);
    *(LAS unsigned*)(L_QA + 4 * lane) = pack2((dec(v[0][0]) + dec(v[1][0])) + (dec(v[2][0]) + dec(v[3][0])), (dec(v[0][2]) + dec(v[1][2])) + (dec(v[2][2]) + dec(v[3][2])));
    lds_signal(C_QA);
    STAMP(10);
  }
  raise_flag(p.ws, WS_FQ + (j * 4 + w) * 8, t);
  if (w == 1 && (j & 3) == ((j >> 2) & 3)) {
    const int j0 = j & ~3;
    wait_flags(p.ws, WS_FQ + j0 * 32 + c * 8, t);
    u32x4 v[4]; fetch<true>(v, p.ws, WS_QB1 + j0 * 1024 + lane * 16, 1024);
    const f32x4 q4 = (asf(v[0]) + asf(v[1])) + (asf(v[2]) + asf(v[3]));
    const unsigned p01 = pack2(q4[0], q4[1]), p23 = pack2(q4[2], q4[3]);
    const int bb = lane & 3;
    const unsigned x0 = dppu<0x00>(p01), x1 = dppu<0x55>(p01), x2 = dppu<0xAA>(p01), x3 = dppu<0xFF>(p01);
    const unsigned y0 = dppu<0x00>(p23), y1 = dppu<0x55>(p23), y2 = dppu<0xAA>(p23), y3 = dppu<0xFF>(p23);
    const unsigned e[4] = {(bb & 2) ? y0 : x0, (bb & 2) ? y1 : x1, (bb & 2) ? y2 : x2, (bb & 2) ? y3 : x3};
    const u32x2 o = (bb & 1) ? u32x2{(e[0] >> 16) | (e[1] & 0xffff0000u), (e[2] >> 16) | (e[3] & 0xffff0000u)}
                             : u32x2{(e[0] & 0xffffu) | (e[1] << 16), (e[2] & 0xffffu) | (e[3] << 16)};
    st8(p.ws, WS_QB2 + (bb * HD + (j >> 2) * 64 + (lane & ~3)) * 2, o);
    raise_flag(p.ws, WS_FQ2 + (j >> 2) * 8, t);
  }
  if (w == 2) {
    wait_flags(p.ws, WS_FQ2 + (((j >> 4) & 3) * 16 + c) * 8, t);
    u32x4 v[2]; fetch<false>(v, p.ws, WS_QB2 + (j >> 4) * 2048 + lane * 16, 1024);
#pragma unroll
    for (int i = 0; i < 2; ++i) *(LAS u32x4*)(L_QB + (4 * i + g) * 272 + 16 * c) = v[i];
    lds_signal(C_QB);
  }
  lds_wait(C_QB, 1);
  lds_wait(C_KV, 4);
  if (w == 0) STAMP(4);
  {
    u32x4 Q[4];
#pragma unroll
    for (int kk = 0; kk < 4; ++kk) Q[kk] = lds128(L_QB + (c & 7) * 272 + 64 * kk + 16 * g);
    float s[8][4]; float mx = -INFINITY;
#pragma unroll
    for (int blk = 0; blk < 8; ++blk) {
      f32x4 d = {0.f, 0.f, 0.f, 0.f};
#pragma unroll
      for (int kk = 0; kk < 4; ++kk) d = mfma16(lds128(L_KB + (16 * blk + c) * 256 + 16 * ((4 * kk + g) ^ ((c & 3) << 2))), Q[kk], d);
#pragma unroll
      for (int r = 0; r < 4; ++r) { s[blk][r] = bf16r(bf16r(d[r]) * SCALE); mx = fmaxf(mx, s[blk][r]); }
    }
    mx = x32_max(x16_max(mx));
    const float mL = mulL(mx);
    float l = 0.f;
#pragma unroll
    for (int blk = 0; blk < 8; ++blk)
#pragma unroll
      for (int r = 0; r < 4; ++r) { s[blk][r] = ex2(__builtin_fmaf(s[blk][r], LOG2E, -mL)); l += s[blk][r]; }
    l = x32_sum(x16_sum(l));
    f32x4 o0 = {0.f, 0.f, 0.f, 0.f}, o1 = {0.f, 0.f, 0.f, 0.f};
    const int q = (lane >> 2) & 3, pp = lane & 3;
    const unsigned va0 = L_VB + (4 * g + q) * 256 + 16 * ((4 * w + (pp >> 1)) ^ (q << 2)) + 8 * (pp & 1);
#pragma unroll
    for (int tt = 0; tt < 4; ++tt) {
      const u32x4 P = {pack2(s[2 * tt][0], s[2 * tt][1]), pack2(s[2 * tt][2], s[2 * tt][3]), pack2(s[2 * tt + 1][0], s[2 * tt + 1][1]), pack2(s[2 * tt + 1][2], s[2 * tt + 1][3])};
      const u32x2 a0 = lds_tr(va0 + tt * 8192), a1 = lds_tr(va0 + tt * 8192 + 4096), b0 = lds_tr(va0 + tt * 8192 + 32), b1 = lds_tr(va0 + tt * 8192 + 4096 + 32);
      o0 = mfma16(u32x4{a0[0], a0[1], a1[0], a1[1]}, P, o0);
      o1 = mfma16(u32x4{b0[0], b0[1], b1[0], b1[1]}, P, o1);
    }
    const int rec = ((j >> 4) * 8 + (c & 7)) * 16 + (j & 15);
    st16<true>(p.ws, WS_PB + rec * 512 + (32 * w + 16 * (c >> 3) + 4 * g) * 4, asu(c < 8 ? o0 : o1));
    if (w == 0 && lane < 8) st8(p.ws, WS_PBML + rec * 8, u32x2{__float_as_uint(mL), __float_as_uint(l)});
    raise_flag(p.ws, WS_FP + (j * 4 + w) * 8, t);
  }
  if (w == 3) {
    wait_flags(p.ws, WS_FP + (j >> 4) * 512 + lane * 8, t);
    u32x2 ml = {0u, 0u};
    ld8(ml, p.ws, WS_PBML + ((j >> 1) * 16 + 4 * (lane & 3) + g) * 8);
    u32x4 v[4]; fetch<true>(v, p.ws, WS_PB + ((j >> 1) * 16 + g) * 512 + (j & 1) * 256 + c * 16, 2048);
    pin(ml);
    const float m = __uint_as_float(ml[0]);
    float M = fmaxf(m, dppf<XOR1>(m)); M = fmaxf(M, dppf<XOR2>(M)); M = x32_max(x16_max(M));
    const float wt = ex2(m - M);
    float L = wt * __uint_as_float(ml[1]); L += dppf<XOR1>(L); L += dppf<XOR2>(L); L = x32_sum(x16_sum(L));
    f32x4 a = asf(v[0]) * dppf<0x00>(wt) + asf(v[1]) * dppf<0x55>(wt) + asf(v[2]) * dppf<0xAA>(wt) + asf(v[3]) * dppf<0xFF>(wt);
#pragma unroll
    for (int r = 0; r < 4; ++r) a[r] = x32_sum(x16_sum(a[r])) / L;
    const int slot = 8 * ((j >> 1) & 31) + 2 * (j >> 6) + (j & 1);
    if (lane < 16) st8(p.ws, WS_OB + slot * 128 + c * 8, u32x2{pack2(a[0], a[1]), pack2(a[2], a[3])});
    raise_flag(p.ws, WS_FO + slot * 8, t);
  }
  if (w == 2) {
    wait_flags(p.ws, WS_FO + ((j & ~15) + c) * 8, t);
    u32x4 v[2]; fetch<false>(v, p.ws, WS_OB + (j >> 4) * 2048 + lane * 16, 1024);
#pragma unroll
    for (int i = 0; i < 2; ++i) *(LAS u32x4*)(L_OB + g * 576 + 256 * i + 16 * c) = v[i];
    lds_signal(C_OB);
  }
  lds_wait(C_OB, 1);
  lds_wait(C_WOB, 4);
  {
    load_wfrag(F, L_R1, w, lane);
    const f32x4 d = piece(L_OB, 576, lane, F);
    const int cb16 = 4 * (j & 15) + w;
    if (g == 0) st16<true>(p.ws, WS_YB + cb16 * 4096 + (j >> 4) * 256 + c * 16, asu(d));
    raise_flag(p.ws, WS_FY + cb16 * 128 + (j >> 4) * 8, t);
  }
  if (w == 1 && (j & 3) == ((j >> 2) & 3)) {
    wait_flags(p.ws, WS_FY + (j >> 2) * 128 + c * 8, t);
    u32x4 v[4]; fetch<true>(v, p.ws, WS_YB + (j >> 2) * 4096 + lane * 16, 1024);
    f32x4 a = (asf(v[0]) + asf(v[1])) + (asf(v[2]) + asf(v[3]));
#pragma unroll
    for (int r = 0; r < 4; ++r) a[r] = x32_sum(x16_sum(a[r]));
    p.out[g * 2 * DM + DM + 16 * (j >> 2) + c] = f2bf(sel4(a, g));
  }
  if (w == 1) STAMP(5);
  load_wfrag(F, L_R0, w, lane);
#pragma unroll
  for (int kk = 0; kk < 8; ++kk) pin(F[kk]);
  waitvm<0>();
  u64 on = DRY ? 0ull : ~0ull;
#pragma unroll 1
  while (true) {
    asm volatile("" : "+s"(on));
    const bool live = on != 0;
    asm volatile("s_waitcnt lgkmcnt(0)" ::: "memory");
    if (live) __builtin_amdgcn_s_barrier();
    asm volatile("" ::: "memory");
    if (w == 0) {
      STAMP(6);
      f32x2 ml[4], oo[4];
#pragma unroll
      for (int i = 0; i < 4; ++i) { ml[i] = lds64f(L_PART + i * 528); oo[i] = lds64f(L_PART + i * 528 + 16 + 8 * lane); }
      const float M = fmaxf(fmaxf(ml[0][0], ml[1][0]), fmaxf(ml[2][0], ml[3][0])), ML = mulL(M);
      float L = 0.f, a0 = 0.f, a1 = 0.f;
#pragma unroll
      for (int i = 0; i < 4; ++i) { const float wt = ex2(mulL(ml[i][0]) - ML); L = __builtin_fmaf(wt, ml[i][1], L); a0 = __builtin_fmaf(wt, oo[i][0], a0); a1 = __builtin_fmaf(wt, oo[i][1], a1); }
      const unsigned rec = WS_PA + (8 * ((ja >> 1) & 31) + 2 * (ja >> 6) + (ja & 1)) * PA_REC;
      put2(p.ws, rec + lane * 16, a0, a1, t, on);
      put2(p.ws, rec + 1024 + (lane & 7) * 16, M, L, t, on & 0xffull);
    }
    {
      u32x4 v[8]; unsigned off[8];
#pragma unroll
      for (int i = 0; i < 4; ++i) { const unsigned rec = WS_PA + ((j & ~15) + 8 * (i >> 1) + 2 * w + (i & 1)) * PA_REC; off[i] = rec + lane * 16; off[4 + i] = rec + 1024 + (lane & 7) * 16; }
      gather16<8, PA_SLEEP>(v, p.ws, off, t, on);
      if (w == 0) STAMP(7);
#pragma unroll
      for (int hh = 0; hh < 2; ++hh) {
        const float m0 = dec(v[4 + 2 * hh][0]), m1 = dec(v[5 + 2 * hh][0]), ML = mulL(fmaxf(m0, m1));
        const float w0 = ex2(mulL(m0) - ML), w1 = ex2(mulL(m1) - ML);
        const float L = w0 * dec(v[4 + 2 * hh][2]) + w1 * dec(v[5 + 2 * hh][2]);
        const float a0 = w0 * dec(v[2 * hh][0]) + w1 * dec(v[2 * hh + 1][0]), a1 = w0 * dec(v[2 * hh][2]) + w1 * dec(v[2 * hh + 1][2]);
        const float rl = __builtin_amdgcn_rcpf(L);
        *(LAS unsigned*)(L_OA + w * 576 + 256 * hh + 4 * lane) = pack2(a0 * rl, a1 * rl);
      }
      asm volatile("s_waitcnt lgkmcnt(0)" ::: "memory");
      if (live) __builtin_amdgcn_s_barrier();
      asm volatile("" ::: "memory");
      const f32x4 d = piece(L_OA, 576, lane, F);
      const int n = 64 * (j & 15) + 16 * w + c;
      put2(p.ws, WS_YA + ((g & 1) * 128 + (n >> 3)) * 2048 + (j >> 4) * 128 + (n & 7) * 16, g ? d[2] : d[0], g ? d[3] : d[1], t, on & 0xffffffffull);
    }
    if (w == 0) {
      STAMP(8);
      u32x4 v[2]; const unsigned off[2] = {WS_YA + j * 2048 + lane * 16, WS_YA + j * 2048 + 1024 + lane * 16};
      gather16<2, Y_SLEEP>(v, p.ws, off, t, on);
      float a = dec(v[0][0]) + dec(v[1][0]), b = dec(v[0][2]) + dec(v[1][2]);
      a += dppf<ROR8>(a); b += dppf<ROR8>(b);
      a = x32_sum(x16_sum(a)); b = x32_sum(x16_sum(b));
      u64 keep;
      asm volatile("s_mov_b64 %0, exec\n\ts_mov_b64 exec, %4\n\tglobal_store_short %1, %2, %3\n\ts_mov_b64 exec, %0"
                   : "=&s"(keep) : "v"(((2 * (j >> 7) + ((lane >> 3) & 1)) * 2 * DM + 8 * (j & 127) + (lane & 7)) * 2), "v"((unsigned)f2bf(lane < 8 ? a : b)), "s"(p.out), "s"(on & 0xffffull) : "memory");
      STAMP(9);
    }
    if (live) break;
    on = ~0ull;
  }
  if (w == 0) {
    if (j == 0 && lane == 0) __hip_atomic_store((GAS u64*)(p.ws + WS_EPOCH), epoch + 1, __ATOMIC_RELAXED, __HIP_MEMORY_SCOPE_AGENT);
  }
}

extern "C" void kernel_launch(void* const* d_in, const int* in_sizes, int n_in, void* d_out, int out_size, void* d_ws, size_t ws_size, hipStream_t stream) {
  Params p;
  p.xA = (const u16*)d_in[0]; p.kA = (const u16*)d_in[1]; p.vA = (const u16*)d_in[2]; p.wqA = (const u16*)d_in[3]; p.woA = (const u16*)d_in[4];
  p.xB = (const u16*)d_in[5]; p.kB = (const u16*)d_in[6]; p.vB = (const u16*)d_in[7]; p.wqB = (const u16*)d_in[8]; p.woB = (const u16*)d_in[9];
  p.out = (u16*)d_out; p.ws = (unsigned char*)d_ws;
  (void)hipFuncSetAttribute((const void*)fused_decode, hipFuncAttributeMaxDynamicSharedMemorySize, LDS_BYTES);
  fused_decode<<<NWG, NTHR, LDS_BYTES, stream>>>(p);
}
```

```cpp
#include <hip/hip_runtime.h>
#include <hip/hip_bf16.h>
#include <cstdint>
#pragma clang diagnostic ignored "-Wint-to-pointer-cast"

typedef __attribute__((ext_vector_type(4))) unsigned u32x4;
typedef __attribute__((ext_vector_type(2))) unsigned u32x2;
typedef __attribute__((ext_vector_type(4))) float f32x4;
typedef __attribute__((ext_vector_type(2))) float f32x2;
typedef __attribute__((ext_vector_type(8))) __bf16 bf16x8;
typedef __attribute__((ext_vector_type(2))) __bf16 bf16x2;
typedef __attribute__((ext_vector_type(4))) short s16x4;
typedef unsigned short u16;
typedef unsigned long long u64;
#define LAS __attribute__((address_space(3)))
#define GAS __attribute__((address_space(1)))
#define INL __device__ __forceinline__

constexpr int DM = 1024, NH = 32, DH = 128, SK = 8192, WIN = 2048, HD = NH * DH;
constexpr float SCALE = 0.088388347648318440f, LOG2E = 1.4426950408889634f;
constexpr int NWG = 256, NTHR = 512;

#define SCH 1750
#define SCH0 740
#define DLO 4
#define DHI 7
#define DSH 6
#define MAPO 1
#define MAPA 0
#define MAPB 0
#define PA_SLEEP 1
#define Y_SLEEP 0
#define TOUCH 1
#define LATER 16
#define PW 5
#define ENDR 0
#define QA_SLEEP 8
#define NAP_Q 6
#define NAP_M 12
#define NAP_O 18

constexpr unsigned WS_EPOCH = 0;
constexpr unsigned WS_QA = 4096;
constexpr unsigned PA_REC = 1152;
constexpr unsigned WS_PA = WS_QA + NWG * 2048;
constexpr unsigned WS_YA = WS_PA + NWG * PA_REC;
constexpr unsigned WS_QB = WS_YA + NWG * 2048;
constexpr unsigned PB_REC = 4096 + 128;
constexpr unsigned WS_PB = WS_QB + 64 * 1024;
constexpr unsigned WS_OB = WS_PB + 128 * PB_REC;
constexpr unsigned WS_YB = WS_OB + 128 * 512;
constexpr unsigned WS_END = WS_YB + 64 * 2048;
static_assert(WS_END <= (4u << 20), "workspace");

constexpr unsigned L_R0 = 0, L_R1 = 32768, L_R2 = 65536, L_R3 = 98304;
constexpr unsigned L_QA = 131072;
constexpr unsigned L_OA = L_QA + 256;
constexpr unsigned L_PART = L_OA + 4 * 576;
constexpr unsigned L_XA = L_PART + 64 + 4 * 512;
constexpr unsigned L_VB = L_XA + 2048;
constexpr unsigned L_SC = L_VB + 4 * 2112;
constexpr unsigned L_CNT = L_SC + 8192;
constexpr unsigned LDS_BYTES = L_CNT + 64 + 256;
enum { C_WOA = 0, C_B, C_FREE0, C_FREE1, C_QA, C_VB, C_PB, C_AGG, C_AGG2, C_LATE };
constexpr unsigned SPIN_CAP = 1u << 17;

#define STAMP(i) do { } while (0)

struct Params {
  const u16 *wqA, *xA, *kA, *vA, *woA, *xB, *kB, *vB, *wqB, *woB;
  u16* out;
  unsigned char* ws;
};

template <int V> struct IC { static constexpr int value = V; };
template <int A, int B, typename F> INL void static_for(F&& f) { if constexpr (A < B) { f(IC<A>{}); static_for<A + 1, B>(f); } }
INL float bf2f(u16 v) { return __uint_as_float(((unsigned)v) << 16); }
INL u16 f2bf(float f) { return __builtin_bit_cast(u16, (__bf16)f); }
INL float bf16r(float f) { return bf2f(f2bf(f)); }
INL unsigned pack2(float lo, float hi) { return __builtin_bit_cast(unsigned, __builtin_convertvector(f32x2{lo, hi}, bf16x2)); }
INL float lo16(unsigned u) { return __uint_as_float(u << 16); }
INL float hi16(unsigned u) { return __uint_as_float(u & 0xffff0000u); }
template <int CTRL> INL float dppf(float x) { return __builtin_bit_cast(float, __builtin_amdgcn_mov_dpp(__builtin_bit_cast(int, x), CTRL, 0xf, 0xf, true)); }
template <int CTRL> INL unsigned dppu(unsigned x) { return (unsigned)__builtin_amdgcn_mov_dpp((int)x, CTRL, 0xf, 0xf, true); }
constexpr int XOR1 = 0xB1, XOR2 = 0x4E, ROR4 = 0x124, ROR8 = 0x128, BCAST = 0x150;
INL float x16_max(float x) { auto s = __builtin_amdgcn_permlane16_swap(__float_as_uint(x), __float_as_uint(x), false, false); return fmaxf(__uint_as_float(s[0]), __uint_as_float(s[1])); }
INL float x32_max(float x) { auto s = __builtin_amdgcn_permlane32_swap(__float_as_uint(x), __float_as_uint(x), false, false); return fmaxf(__uint_as_float(s[0]), __uint_as_float(s[1])); }
INL float x16_sum(float x) { auto s = __builtin_amdgcn_permlane16_swap(__float_as_uint(x), __float_as_uint(x), false, false); return __uint_as_float(s[0]) + __uint_as_float(s[1]); }
INL float x32_sum(float x) { auto s = __builtin_amdgcn_permlane32_swap(__float_as_uint(x), __float_as_uint(x), false, false); return __uint_as_float(s[0]) + __uint_as_float(s[1]); }
INL f32x4 mfma16(u32x4 a, u32x4 b, f32x4 c) { return __builtin_amdgcn_mfma_f32_16x16x32_bf16(__builtin_bit_cast(bf16x8, a), __builtin_bit_cast(bf16x8, b), c, 0, 0, 0); }
INL float ex2(float x) { return __builtin_amdgcn_exp2f(x); }
INL float mulL(float m) { float r = m * LOG2E; asm volatile("" : "+v"(r)); return r; }
INL u32x4 asu(const f32x4& v) { return __builtin_bit_cast(u32x4, v); }
INL f32x4 asf(const u32x4& v) { return __builtin_bit_cast(f32x4, v); }
INL float sel4(const f32x4& d, int g) { return (g & 2) ? ((g & 1) ? d[3] : d[2]) : ((g & 1) ? d[1] : d[0]); }
template <typename T> INL void pin(T& d) { asm volatile("" : "+v"(d)); }

INL u32x4 lds128(unsigned a) { return *(const LAS u32x4*)a; }
INL f32x2 lds64f(unsigned a) { return *(const LAS f32x2*)a; }
INL u32x2 lds_tr(unsigned a) { return __builtin_bit_cast(u32x2, __builtin_amdgcn_ds_read_tr16_b64_v4i16((LAS s16x4*)a)); }
INL void lds_signal(int c) {
  u64 keep;
  asm volatile("s_waitcnt lgkmcnt(0)\n\ts_mov_b64 %0, exec\n\ts_mov_b64 exec, 1\n\tds_add_u32 %1, %2\n\ts_mov_b64 exec, %0" : "=&s"(keep) : "v"(L_CNT + 4 * c), "v"(1u) : "memory");
}
INL unsigned lds_ticket(int c) {
  unsigned old; u64 keep;
  asm volatile("s_waitcnt lgkmcnt(0)\n\ts_mov_b64 %1, exec\n\ts_mov_b64 exec, 1\n\tds_add_rtn_u32 %0, %2, %3\n\ts_mov_b64 exec, %1\n\ts_waitcnt lgkmcnt(0)"
               : "=&v"(old), "=&s"(keep) : "v"(L_CNT + 4 * c), "v"(1u) : "memory");
  return __builtin_amdgcn_readfirstlane(old);
}
INL void lds_wait(int c, unsigned n) {
#pragma unroll 1
  for (unsigned s = 0; s < SPIN_CAP * 8; ++s) {
    if ((unsigned)__builtin_amdgcn_readfirstlane(*(volatile LAS unsigned*)(L_CNT + 4 * c)) >= n) break;
    __builtin_amdgcn_s_sleep(1);
  }
  asm volatile("" ::: "memory");
}
INL void wg_barrier() { asm volatile("s_waitcnt lgkmcnt(0)\n\ts_barrier" ::: "memory"); }
INL void nap(int n) {
#pragma unroll 1
  for (int i = 0; i < n; ++i) __builtin_amdgcn_s_sleep(64);
}

template <int OFF> INL void aload(u32x4& d, unsigned voff, const void* sbase) {
  asm volatile("global_load_dwordx4 %0, %1, %2 offset:%3 nt" : "+v"(d) : "v"(voff), "s"(sbase), "i"(OFF) : "memory");
}
template <int N> INL void waitv(u32x4& d) { asm volatile("s_waitcnt vmcnt(%1)" : "+v"(d) : "i"(N) : "memory"); }
template <int N> INL void waitvm() { asm volatile("s_waitcnt vmcnt(%0)" :: "i"(N) : "memory"); }
INL void pacev(u32x4& d, unsigned depth) {
  asm volatile("s_cmp_eq_u32 %1, 7\n\ts_cbranch_scc1 7f\n\ts_cmp_eq_u32 %1, 6\n\ts_cbranch_scc1 6f\n\ts_cmp_eq_u32 %1, 5\n\ts_cbranch_scc1 5f\n\t"
               "s_waitcnt vmcnt(4)\n5:\n\ts_waitcnt vmcnt(5)\n6:\n\ts_waitcnt vmcnt(6)\n7:\n\ts_waitcnt vmcnt(7)" : "+v"(d) : "s"(depth) : "memory", "scc");
}
template <int W, int OFF> INL void stage(u32x4& d, unsigned lds_addr) {
  asm volatile("s_waitcnt vmcnt(%2)\n\tds_write_b128 %1, %0 offset:%3" : "+v"(d) : "v"(lds_addr), "i"(W), "i"(OFF) : "memory");
}

INL float dec(unsigned v) { return __uint_as_float(v); }
INL void put2(const unsigned char* ws, unsigned off, float a, float b, unsigned tag, u64 mask) {
  u64 keep;
  asm volatile("s_mov_b64 %0, exec\n\ts_mov_b64 exec, %4\n\tglobal_store_dwordx4 %1, %2, %3 sc1\n\ts_mov_b64 exec, %0\n\ts_nop 0"
               : "=&s"(keep) : "v"(off), "v"(u32x4{__float_as_uint(a), tag, __float_as_uint(b), tag}), "s"(ws), "s"(mask) : "memory");
}
INL void ld16m(u32x4& d, const unsigned char* ws, unsigned off, u64 mask) {
  asm volatile("s_mov_b64 exec, %3\n\tglobal_load_dwordx4 %0, %1, %2 sc1\n\ts_mov_b64 exec, -1" : "+v"(d) : "v"(off), "s"(ws), "s"(mask) : "memory");
}
template <int N, int SLEEP> INL void gather(u32x4 (&v)[N], const unsigned char* ws, const unsigned (&off)[N], unsigned tag) {
  u64 need[N];
#pragma unroll
  for (int i = 0; i < N; ++i) { asm volatile("" : "=v"(v[i])); need[i] = ~0ull; }
#pragma unroll 1
  for (unsigned s = 0; s < SPIN_CAP; ++s) {
#pragma unroll
    for (int i = 0; i < N; ++i) ld16m(v[i], ws, off[i], need[i]);
    waitvm<0>();
    unsigned bad[N], any = 0;
#pragma unroll
    for (int i = 0; i < N; ++i) pin(v[i]);
#pragma unroll
    for (int i = 0; i < N; ++i) { bad[i] = (v[i][1] ^ tag) | (v[i][3] ^ tag); any |= bad[i]; }
    pin(any);
    if (__builtin_amdgcn_ballot_w64(any != 0) == 0) break;
#pragma unroll
    for (int i = 0; i < N; ++i) {
      const u64 m = __builtin_amdgcn_ballot_w64(bad[i] != 0);
      asm volatile("s_cmp_lg_u64 %1, 0\n\ts_cselect_b64 %0, -1, 0" : "=s"(need[i]) : "s"(m) : "scc");
    }
    if (SLEEP) __builtin_amdgcn_s_sleep(SLEEP);
  }
}
INL void publish256(const unsigned char* ws, unsigned off, int cnt, int lane, unsigned tag) {
  if (lds_ticket(cnt) == 3) {
    const f32x2 e0 = lds64f(L_OA + lane * 8), e1 = lds64f(L_OA + 512 + lane * 8);
    put2(ws, off + lane * 16, e0[0], e0[1], tag, ~0ull);
    put2(ws, off + 1024 + lane * 16, e1[0], e1[1], tag, ~0ull);
  }
}
INL void store_bf16(u16* out, unsigned byte_off, float v) { asm volatile("global_store_short %0, %1, %2 sc1" :: "v"(byte_off), "v"(pack2(v, v)), "s"(out) : "memory"); }

INL unsigned score_step(u32x4 (&R)[8], const u32x4 (&QB)[4], unsigned voff, const char* next, unsigned depth) {
  f32x4 s[2] = {{0.f, 0.f, 0.f, 0.f}, {0.f, 0.f, 0.f, 0.f}};
  static_for<0, 8>([&](auto ic) __attribute__((always_inline)) {
    constexpr int i = decltype(ic)::value;
    pacev(R[i], depth);
    s[i >> 2] = mfma16(R[i], QB[i & 3], s[i >> 2]);
    __builtin_amdgcn_sched_barrier(0);
    aload<1024 * i - 4096>(R[i], voff, next);
    __builtin_amdgcn_sched_barrier(0);
  });
  float d = dppf<0x00>(s[0][0]); d += dppf<0x55>(s[0][1]); d += dppf<0xAA>(s[0][2]); d += dppf<0xFF>(s[0][3]);
  float e = dppf<0x00>(s[1][0]); e += dppf<0x55>(s[1][1]); e += dppf<0xAA>(s[1][2]); e += dppf<0xFF>(s[1][3]);
  const unsigned u = pack2(d, e);
  return pack2(lo16(u) * SCALE, hi16(u) * SCALE);
}
template <bool MORE>
INL void value_step(f32x4& acc, float& l, float mL, unsigned sc, u32x4 (&R)[8], const unsigned (&msk)[4], unsigned voff, const char* next, unsigned depth) {
  const float pa = ex2(__builtin_fmaf(lo16(sc), LOG2E, -mL)), pb = ex2(__builtin_fmaf(hi16(sc), LOG2E, -mL));
  l += pa + pb;
  unsigned pk[2] = {pack2(pa, pa), pack2(pb, pb)};
  pin(pk[0]); pin(pk[1]);
  static_for<0, 8>([&](auto ic) __attribute__((always_inline)) {
    constexpr int i = decltype(ic)::value;
    const unsigned b = pk[i >> 2];
    const u32x4 P = {dppu<BCAST + 4 * (i & 3)>(b) & msk[0], dppu<BCAST + 4 * (i & 3)>(b) & msk[1], dppu<BCAST + 4 * (i & 3)>(b) & msk[2], dppu<BCAST + 4 * (i & 3)>(b) & msk[3]};
    if constexpr (MORE) pacev(R[i], depth); else waitv<7 - i>(R[i]);
    acc = mfma16(R[i], P, acc);
    if constexpr (MORE) { __builtin_amdgcn_sched_barrier(0); aload<1024 * i - 4096>(R[i], voff, next); }
  });
}
INL unsigned pace_depth(unsigned t0, unsigned round) {
  const int late = (int)((unsigned)__builtin_amdgcn_s_memrealtime() - t0 - (SCH0 + ((round * SCH) >> 4)));
  if (ENDR && round > 64 - ENDR) return 7;
  return __builtin_amdgcn_readfirstlane(min(max(((late >> DSH) + 1) + DLO, DLO), DHI));
}

INL void load_wfrag(u32x4 (&B)[8], unsigned region, int w, int lane) {
  const int g = lane >> 4, q = (lane >> 2) & 3, pp = lane & 3;
  const unsigned sw = 2u * ((q >> 1) | ((g & 1) << 1));
  const unsigned a = region + (8 * g + q) * 128 + 16 * ((2 * w + (pp >> 1)) ^ sw) + 8 * (pp & 1);
#pragma unroll
  for (int kk = 0; kk < 8; ++kk) { const u32x2 lo = lds_tr(a + kk * 4096), hi = lds_tr(a + kk * 4096 + 512); B[kk] = u32x4{lo[0], lo[1], hi[0], hi[1]}; }
}
INL f32x4 piece(unsigned a, const u32x4 (&B)[8], f32x4 d) {
  u32x4 A[8];
#pragma unroll
  for (int kk = 0; kk < 8; ++kk) A[kk] = lds128(a + 64 * kk);
  __builtin_amdgcn_sched_barrier(0);
#pragma unroll
  for (int kk = 0; kk < 8; ++kk) d = mfma16(A[kk], B[kk], d);
  return d;
}
INL f32x4 piece4(int lane, int w) {
  f32x4 d = {0.f, 0.f, 0.f, 0.f};
#pragma unroll 1
  for (int tt = 0; tt < 4; ++tt) { u32x4 B[8]; load_wfrag(B, ((tt + 2) & 3) * 32768, w, lane); d = piece(L_VB + tt * 512 + (lane & 3) * 2112 + 16 * (lane >> 4), B, d); }
  return d;
}
INL unsigned wtile_voff(int lane, int odd, unsigned rs) {
  const unsigned sw = 2u * (((lane >> 4) & 1) | (odd << 1));
  return (lane >> 3) * rs + 16 * ((lane & 7) ^ sw);
}

__global__ void __launch_bounds__(NTHR, 2) fused_decode(Params p) {
  const int lane = threadIdx.x & 63, wave = __builtin_amdgcn_readfirstlane(threadIdx.x >> 6);
  const int j = blockIdx.x, g = lane >> 4, c = lane & 15;
  const int jo = ((j & 15) << 4) | (j >> 4);
  const int ja = j;
  const int jq = j ^ 8, qd = j >> 2, job = (j ^ (j >> 3)) & 3, idx = 2 * qd + (job & 1);

  if (wave >= 4) {
    const int w = wave - 4;
    u32x4 xv = {0u, 0u, 0u, 0u};
    if (w < 2) asm volatile("global_load_dwordx4 %0, %1, %2" : "+v"(xv) : "v"((lane >> 5) * DM * 2 + (lane & 31) * 16), "s"((const char*)p.xA + (w * 2 * DM + (j & 3) * 256) * 2) : "memory");
    if (w == 3 && lane < 16) *(LAS unsigned*)(L_CNT + 4 * lane) = 0u;
    u64 epoch;
    asm volatile("s_load_dwordx2 %0, %1, 0x0\n\ts_waitcnt lgkmcnt(0)" : "=s"(epoch) : "s"(p.ws + WS_EPOCH) : "memory");
    u64 z = epoch + 0x9E3779B97F4A7C15ull;
    z = (z ^ (z >> 30)) * 0xBF58476D1CE4E5B9ull; z = (z ^ (z >> 27)) * 0x94D049BB133111EBull; z ^= z >> 31;
    const unsigned tag = (unsigned)(z >> 32);
    if (w < 2) { waitv<0>(xv); *(LAS u32x4*)(L_XA + w * 1024 + lane * 16) = xv; }
    wg_barrier();
    if (w == 0) STAMP(3);
    u32x4 F[8];
    load_wfrag(F, L_R0, w, lane);
    *(LAS float*)(L_OA + (g * 64 + 16 * w + c) * 4) = sel4(piece(L_XA + (lane & 3) * 512 + 16 * g, F, f32x4{0.f, 0.f, 0.f, 0.f}), g);
    publish256(p.ws, WS_QA + jq * 2048, C_AGG, lane, tag);
    lds_signal(C_FREE0);
    lds_wait(C_WOA, 4);
    load_wfrag(F, L_R1, w, lane);
#pragma unroll
    for (int kk = 0; kk < 8; ++kk) pin(F[kk]);
    lds_signal(C_FREE1);
    if (w == 0) {
      u32x4 v[4]; unsigned off[4];
#pragma unroll
      for (int i = 0; i < 4; ++i) off[i] = WS_QA + (8 * ((ja >> 1) & 31) + 4 * (lane >> 5) + i) * 2048 + (ja >> 6) * 512 + (lane & 31) * 16;
      gather<4, QA_SLEEP>(v, p.ws, off, tag);
      *(LAS unsigned*)(L_QA + 4 * lane) = pack2((dec(v[0][0]) + dec(v[1][0])) + (dec(v[2][0]) + dec(v[3][0])), (dec(v[0][2]) + dec(v[1][2])) + (dec(v[2][2]) + dec(v[3][2])));
      lds_signal(C_QA);
      STAMP(10);
    }
    if (job >= 2) {
      {
        nap(NAP_Q);
        u32x4 v[1]; const unsigned off[1] = {WS_QB + (((idx >> 3) & 3) * 16 + 4 * w + g) * 1024 + (idx >> 5) * 256 + c * 16};
        gather<1, 64>(v, p.ws, off, tag);
        *(LAS u32x2*)(L_VB + (2 * w + (lane >> 5)) * 272 + (lane & 31) * 8) = u32x2{v[0][0], v[0][2]};
        lds_signal(C_VB);
      }
      lds_wait(C_VB, 4);
      lds_wait(C_B, 4);
      u32x4 Q[4];
#pragma unroll
      for (int kk = 0; kk < 4; ++kk) Q[kk] = lds128(L_VB + (c & 7) * 272 + 64 * kk + 16 * g);
      float s[16][4]; float mx = -INFINITY;
#pragma unroll
      for (int blk = 0; blk < 16; ++blk) {
        f32x4 d = {0.f, 0.f, 0.f, 0.f};
#pragma unroll
        for (int kk = 0; kk < 4; ++kk) d = mfma16(lds128(L_R2 + (16 * blk + c) * 256 + 16 * ((4 * kk + g) ^ ((c & 3) << 2))), Q[kk], d);
#pragma unroll
        for (int r = 0; r < 4; ++r) { s[blk][r] = bf16r(bf16r(d[r]) * SCALE); mx = fmaxf(mx, s[blk][r]); }
      }
      const float mL = mulL(x32_max(x16_max(mx)));
      float l = 0.f;
#pragma unroll
      for (int blk = 0; blk < 16; ++blk)
#pragma unroll
        for (int r = 0; r < 4; ++r) { s[blk][r] = ex2(__builtin_fmaf(s[blk][r], LOG2E, -mL)); l += s[blk][r]; }
      l = x32_sum(x16_sum(l));
      f32x4 o0 = {0.f, 0.f, 0.f, 0.f}, o1 = {0.f, 0.f, 0.f, 0.f};
      const int q = (lane >> 2) & 3, pp = lane & 3;
      const unsigned va0 = L_R0 + (4 * g + q) * 256 + 16 * ((4 * w + (pp >> 1)) ^ (q << 2)) + 8 * (pp & 1);
#pragma unroll
      for (int tt = 0; tt < 8; ++tt) {
        const u32x4 P = {pack2(s[2 * tt][0], s[2 * tt][1]), pack2(s[2 * tt][2], s[2 * tt][3]), pack2(s[2 * tt + 1][0], s[2 * tt + 1][1]), pack2(s[2 * tt + 1][2], s[2 * tt + 1][3])};
        const u32x2 a0 = lds_tr(va0 + tt * 8192), a1 = lds_tr(va0 + tt * 8192 + 4096), b0 = lds_tr(va0 + tt * 8192 + 32), b1 = lds_tr(va0 + tt * 8192 + 4096 + 32);
        o0 = mfma16(u32x4{a0[0], a0[1], a1[0], a1[1]}, P, o0);
        o1 = mfma16(u32x4{b0[0], b0[1], b1[0], b1[1]}, P, o1);
      }
      asm volatile("global_store_dwordx4 %0, %1, %2 sc1 nt\n\ts_waitcnt vmcnt(0)" :: "v"(WS_PB + idx * PB_REC + w * 1024 + (c & 7) * 128 + (c >> 3) * 64 + g * 16), "v"(asu(c < 8 ? o0 : o1)), "s"(p.ws) : "memory");
      if (lds_ticket(C_PB) == 3) put2(p.ws, WS_PB + idx * PB_REC + 4096 + (lane & 7) * 16, mL, l, tag, 0xffull);
    } else {
      if (job == 0) {
        u32x4 v[2] = {{0u, 0u, 0u, 0u}, {0u, 0u, 0u, 0u}};
        asm volatile("global_load_dwordx4 %0, %2, %3\n\tglobal_load_dwordx4 %1, %2, %3 offset:1024\n\ts_waitcnt vmcnt(0)" : "+v"(v[0]), "+v"(v[1]) : "v"(lane * 16), "s"(p.xB + w * DM) : "memory");
        *(LAS u32x4*)(L_VB + w * 2112 + lane * 16) = v[0]; *(LAS u32x4*)(L_VB + w * 2112 + 1024 + lane * 16) = v[1];
        lds_signal(C_VB);
        lds_wait(C_VB, 4);
        lds_wait(C_B, 4);
        *(LAS u16*)(L_OA + (g * 64 + 16 * w + c) * 2) = f2bf(sel4(piece4(lane, w), g));
        if (lds_ticket(C_AGG2) == 3) { const f32x2 e = lds64f(L_OA + lane * 8); put2(p.ws, WS_QB + qd * 1024 + lane * 16, e[0], e[1], tag, ~0ull); }
      }
      if (w == 3) {
        nap(NAP_M);
        const unsigned rec = WS_PB + ((idx & ~7) + (lane >> 5)) * PB_REC;
        u32x4 ml[1]; const unsigned off[1] = {rec + 2 * (lane & 3) * PB_REC + 4096 + (idx & 7) * 16};
        gather<1, 64>(ml, p.ws, off, tag);
        u32x4 v[4];
        const unsigned po = rec + ((lane >> 3) & 3) * 1024 + (idx & 7) * 128 + (lane & 7) * 16;
        asm volatile("global_load_dwordx4 %0, %4, %8 sc1 nt\n\tglobal_load_dwordx4 %1, %5, %8 sc1 nt\n\tglobal_load_dwordx4 %2, %6, %8 sc1 nt\n\tglobal_load_dwordx4 %3, %7, %8 sc1 nt\n\ts_waitcnt vmcnt(0)"
                     : "=&v"(v[0]), "=&v"(v[1]), "=&v"(v[2]), "=&v"(v[3]) : "v"(po), "v"(po + 2 * PB_REC), "v"(po + 4 * PB_REC), "v"(po + 6 * PB_REC), "s"(p.ws) : "memory");
        const float m = dec(ml[0][0]);
        float M = fmaxf(m, dppf<XOR1>(m)); M = fmaxf(M, dppf<XOR2>(M)); M = x32_max(M);
        const float wt = ex2(m - M);
        float L = wt * dec(ml[0][2]); L += dppf<XOR1>(L); L += dppf<XOR2>(L); L = x32_sum(L);
        f32x4 a = asf(v[0]) * dppf<0x00>(wt) + asf(v[1]) * dppf<0x55>(wt) + asf(v[2]) * dppf<0xAA>(wt) + asf(v[3]) * dppf<0xFF>(wt);
#pragma unroll
        for (int r = 0; r < 4; ++r) a[r] = x32_sum(a[r]) / L;
        put2(p.ws, WS_OB + ((((idx >> 3) & 3) * 4 + (idx >> 5)) * 8 + (idx & 7)) * 512 + (lane & 31) * 16, dec(pack2(a[0], a[1])), dec(pack2(a[2], a[3])), tag, 0xffffffffull);
      }
      if (job == 1) {
        nap(w == 3 ? NAP_O - NAP_M : NAP_O);
        u32x4 v[4]; unsigned off[4];
#pragma unroll
        for (int i = 0; i < 4; ++i) off[i] = WS_OB + ((qd >> 4) * 4 + w) * 4096 + i * 1024 + lane * 16;
        gather<4, 64>(v, p.ws, off, tag);
#pragma unroll
        for (int i = 0; i < 4; ++i) *(LAS u32x2*)(L_VB + w * 2112 + i * 512 + lane * 8) = u32x2{v[i][0], v[i][2]};
        lds_signal(C_VB);
        lds_wait(C_VB, 4);
        lds_wait(C_B, 4);
        const f32x4 d = piece4(lane, w);
        if (g < 2) *(LAS f32x2*)(L_OA + w * 256 + (lane & 31) * 8) = g ? f32x2{d[2], d[3]} : f32x2{d[0], d[1]};
        publish256(p.ws, WS_YB + qd * 2048, C_AGG2, lane, tag);
        if (w == 0) {
          nap(2);
          u32x4 y[2];
          const unsigned base = WS_YB + ((lane >> 5) * 16 + (qd & 15)) * 2048 + (qd >> 4) * 512 + (lane & 31) * 16, yo[2] = {base, base + 32 * 2048};
          gather<2, 64>(y, p.ws, yo, tag);
          const float a = x32_sum(dec(y[0][0]) + dec(y[1][0])), b = x32_sum(dec(y[0][2]) + dec(y[1][2]));
          store_bf16(p.out, ((2 * (g & 1) + (g >> 1)) * 2 * DM + DM + 64 * (qd & 15) + 16 * (qd >> 4) + c) * 2, g < 2 ? a : b);
        }
      }
    }
    if (w == 1) STAMP(5);
    unsigned off[8], yo[2], pa = WS_PA + (8 * ((ja >> 1) & 31) + 2 * (ja >> 6) + (ja & 1)) * PA_REC + (w ? 1024 + (lane & 7) * 16 : lane * 16);
#pragma unroll
    for (int i = 0; i < 4; ++i) { const unsigned rec = WS_PA + ((jo & ~15) + 8 * (i >> 1) + 2 * w + (i & 1)) * PA_REC; off[i] = rec + lane * 16; off[4 + i] = rec + 1024 + (lane & 7) * 16; pin(off[i]); pin(off[4 + i]); }
    const int n = 64 * (jo & 15) + 16 * w + c;
    unsigned ya = WS_YA + ((g & 1) * 128 + (n >> 3)) * 2048 + (jo >> 4) * 128 + (n & 7) * 16;
    unsigned oa = ((2 * (j >> 7) + ((lane >> 3) & 1)) * 2 * DM + 8 * (j & 127) + (lane & 7)) * 2;
    unsigned lp = L_PART + 64 + 8 * lane, lw = L_OA + w * 576 + 4 * lane, lr = L_OA + (lane & 3) * 576 + 16 * g;
    yo[0] = WS_YA + j * 2048 + lane * 16; yo[1] = yo[0] + 1024;
    pin(pa); pin(ya); pin(oa); pin(lp); pin(lw); pin(lr); pin(yo[0]); pin(yo[1]);
    if (TOUCH != 0) {
      if (TOUCH == 2) lds_wait(C_LATE, 1);
      u32x4 dum;
      asm volatile("global_load_dwordx4 %0, %1, %2 sc1\n\ts_waitcnt vmcnt(0)" : "=&v"(dum) : "v"(w < 2 ? pa : yo[w & 1]), "s"(p.ws) : "memory");
    }
    waitvm<0>();
    wg_barrier();
    if (w < 2) {
      if (w == 0) STAMP(6);
      const f32x4 m01 = asf(lds128(L_PART)), m23 = asf(lds128(L_PART + 16));
      f32x2 oo[4];
#pragma unroll
      for (int i = 0; i < 4; ++i) oo[i] = lds64f(lp + i * 512);
      __builtin_amdgcn_sched_barrier(0);
      const float M = fmaxf(fmaxf(m01[0], m01[2]), fmaxf(m23[0], m23[2]));
      const float w0 = ex2(m01[0] - M), w1 = ex2(m01[2] - M), w2 = ex2(m23[0] - M), w3 = ex2(m23[2] - M);
#pragma unroll
      for (int i = 0; i < 4; ++i) pin(oo[i]);
      if (w == 0) put2(p.ws, pa, (w0 * oo[0][0] + w1 * oo[1][0]) + (w2 * oo[2][0] + w3 * oo[3][0]), (w0 * oo[0][1] + w1 * oo[1][1]) + (w2 * oo[2][1] + w3 * oo[3][1]), tag, ~0ull);
      else put2(p.ws, pa, M, (w0 * m01[1] + w1 * m01[3]) + (w2 * m23[1] + w3 * m23[3]), tag, 0xffull);
    }
    u32x4 v[8];
    gather<8, PA_SLEEP>(v, p.ws, off, tag);
#pragma unroll
    for (int hh = 0; hh < 2; ++hh) {
      const float m0 = dec(v[4 + 2 * hh][0]), m1 = dec(v[5 + 2 * hh][0]), M = fmaxf(m0, m1);
      const float w0 = ex2(m0 - M), w1 = ex2(m1 - M);
      const float rl = __builtin_amdgcn_rcpf(w0 * dec(v[4 + 2 * hh][2]) + w1 * dec(v[5 + 2 * hh][2]));
      const float a0 = w0 * dec(v[2 * hh][0]) + w1 * dec(v[2 * hh + 1][0]), a1 = w0 * dec(v[2 * hh][2]) + w1 * dec(v[2 * hh + 1][2]);
      *(LAS unsigned*)(lw + 256 * hh) = pack2(a0 * rl, a1 * rl);
    }
    wg_barrier();
    const f32x4 d = piece(lr, F, f32x4{0.f, 0.f, 0.f, 0.f});
    put2(p.ws, ya, g ? d[2] : d[0], g ? d[3] : d[1], tag, 0xffffffffull);
    if (w == 0) {
      STAMP(8);
      u32x4 y[2];
      gather<2, Y_SLEEP>(y, p.ws, yo, tag);
      const float a = dec(y[0][0]) + dec(y[1][0]), b = dec(y[0][2]) + dec(y[1][2]);
      const float k = (lane & 8) ? b : a, sd = (lane & 8) ? a : b;
      const float r = x32_sum(x16_sum(k + dppf<ROR8>(sd)));
      if (lane < 16) store_bf16(p.out, oa, r);
      STAMP(9);
      if (j == 0 && lane == 0) __hip_atomic_store((GAS u64*)(p.ws + WS_EPOCH), epoch + 1, __ATOMIC_RELAXED, __HIP_MEMORY_SCOPE_AGENT);
    }
    return;
  }

  if (wave == 0) STAMP(0);
  const unsigned vq0 = wtile_voff(lane, 0, HD * 2), vq1 = wtile_voff(lane, 1, HD * 2);
  const size_t qoff = ((size_t)((j & 3) * 256 + wave * 64) * HD + (jq >> 2) * 64) * 2;
  u32x4 R[8];
  static_for<0, 8>([&](auto ic) __attribute__((always_inline)) {
    constexpr int i = decltype(ic)::value;
    u32x4& r = R[i];
    asm volatile("" : "=v"(r));
    aload<0>(r, (i & 1) ? vq1 : vq0, (const char*)p.wqA + qoff + (size_t)i * 8 * HD * 2);
  });
  const unsigned t0 = (unsigned)__builtin_amdgcn_s_memrealtime();
  int jb = job; asm volatile("" : "+s"(jb));
  const unsigned vo0 = wtile_voff(lane, 0, DM * 2), vo1 = wtile_voff(lane, 1, DM * 2), vkv = g * 256 + 16 * (c ^ (g << 2));
  const size_t ooff = ((size_t)((jo >> 4) * 256 + wave * 64) * DM + (jo & 15) * 64) * 2;
  const unsigned vb0 = jb == 0 ? vq0 : jb == 1 ? vo0 : vkv, vb1 = jb == 0 ? vq1 : jb == 1 ? vo1 : vkv;
  unsigned bstep = jb == 0 ? 8 * HD * 2 : jb == 1 ? 8 * DM * 2 : 1024;
  asm volatile("" : "+s"(bstep));
  const unsigned boff = (jb == 0 ? qd * 128 : jb == 1 ? ((qd >> 4) * 1024 * DM + (qd & 15) * 64) * 2
                                                      : ((idx >> 3) * SK + (SK - WIN) + (idx & 7) * 256) * DH * 2) + wave * 8 * bstep;
  const char* bp = (const char*)(jb == 0 ? p.wqB : jb == 1 ? p.woB : p.kB) + boff;
  const char* const b23 = jb < 2 ? bp + 64 * bstep : (const char*)p.vB + boff;
  const unsigned voffV = lane * 16, voffK = ((lane >> 2) & 3) * 256 + g * 64 + (lane & 3) * 16;
  const unsigned ldst = wave * 8192 + lane * 16;
  const char* const kp = (const char*)p.kA + (size_t)ja * 1048576 + wave * 8192 + 4096;
  const char* const vp = (const char*)p.vA + (size_t)ja * 1048576 + wave * 8192 + 4096;
  static_for<8, 56>([&](auto nc) __attribute__((always_inline)) {
    constexpr int n = decltype(nc)::value, k = n >> 3, i = n & 7;
    constexpr unsigned reg = ((k - 1) & 3) * 32768;
    if constexpr (n == 40) lds_wait(C_FREE0, 4);
    if constexpr (n == 48) lds_wait(C_FREE1, 4);
    stage<n < 16 ? 7 : PW, (reg & 0xffffu) + i * 1024>(R[i], ldst + (reg & ~0xffffu));
    if constexpr (k == 1) aload<0>(R[i], (i & 1) ? vo1 : vo0, (const char*)p.woA + ooff + (size_t)i * 8 * DM * 2);
    if constexpr (k >= 2 && k <= 5) {
      if constexpr (n == 32) bp = b23;
      aload<0>(R[i], (i & 1) ? vb1 : vb0, bp);
      bp += (i == 7 ? 25 : 1) * bstep;
    }
    if constexpr (k == 6) aload<1024 * i - 4096>(R[i], voffK, kp);
    if constexpr (n == 15) wg_barrier();
    if constexpr (n == 23) lds_signal(C_WOA);
    if constexpr (n == 55) lds_signal(C_B);
  });
  if (wave == 0) STAMP(1);
  lds_wait(C_QA, 1);
  if (wave == 0) STAMP(2);
  const unsigned sca = L_SC + wave * 2048 + (lane >> 2) * 4;
  float mx = -INFINITY;
  {
    u32x4 QB[4];
    const u32x4 qv = lds128(L_QA + 16 * (4 * g + (lane & 3)));
#pragma unroll
    for (int i = 0; i < 4; ++i) QB[i] = ((c >> 2) == i) ? qv : u32x4{0u, 0u, 0u, 0u};
#pragma unroll 1
    for (unsigned r = 1; r <= 32; ++r) {
      const unsigned sc = score_step(R, QB, r == 32 ? voffV : voffK, r == 32 ? vp : kp + r * 32768, pace_depth(t0, r));
      *(LAS unsigned*)(sca + (r - 1) * 64) = sc;
      mx = fmaxf(mx, fmaxf(lo16(sc), hi16(sc)));
    }
  }
  mx = fmaxf(mx, dppf<ROR4>(mx)); mx = fmaxf(mx, dppf<ROR8>(mx));
  const float mL = mulL(x32_max(x16_max(mx)));
  if (wave == 0) STAMP(4);
  unsigned msk[4];
#pragma unroll
  for (int i = 0; i < 4; ++i) msk[i] = (c == 2 * i) ? 0x0000ffffu : (c == 2 * i + 1) ? 0xffff0000u : 0u;
  f32x4 acc = {0.f, 0.f, 0.f, 0.f}; float l = 0.f;
#pragma unroll 1
  for (unsigned r = 1; r < 32; ++r) {
    if (TOUCH == 2 && r == LATER && wave == 0) *(volatile LAS unsigned*)(L_CNT + 4 * C_LATE) = 1u;
    value_step<true>(acc, l, mL, *(const LAS unsigned*)(sca + (r - 1) * 64), R, msk, voffV, vp + r * 32768, pace_depth(t0, 32 + r));
  }
  value_step<false>(acc, l, mL, *(const LAS unsigned*)(sca + 31 * 64), R, msk, 0u, nullptr, 0u);
  l += dppf<ROR4>(l); l += dppf<ROR8>(l); l = x32_sum(x16_sum(l));
  if (c < 8) {
#pragma unroll
    for (int r = 0; r < 4; ++r) *(LAS float*)(L_PART + 64 + wave * 512 + (8 * (4 * g + r) + c) * 4) = acc[r];
  }
  if (lane == 0) *(LAS f32x2*)(L_PART + wave * 8) = f32x2{mL, l};
  wg_barrier();
}

extern "C" void kernel_launch(void* const* d_in, const int* in_sizes, int n_in, void* d_out, int out_size, void* d_ws, size_t ws_size, hipStream_t stream) {
  Params p;
  p.xA = (const u16*)d_in[0]; p.kA = (const u16*)d_in[1]; p.vA = (const u16*)d_in[2]; p.wqA = (const u16*)d_in[3]; p.woA = (const u16*)d_in[4];
  p.xB = (const u16*)d_in[5]; p.kB = (const u16*)d_in[6]; p.vB = (const u16*)d_in[7]; p.wqB = (const u16*)d_in[8]; p.woB = (const u16*)d_in[9];
  p.out = (u16*)d_out; p.ws = (unsigned char*)d_ws;
  (void)hipFuncSetAttribute((const void*)fused_decode, hipFuncAttributeMaxDynamicSharedMemorySize, LDS_BYTES);
  fused_decode<<<NWG, NTHR, LDS_BYTES, stream>>>(p);
}
```

```cpp
#include <hip/hip_runtime.h>
#include <hip/hip_bf16.h>
#include <cstdint>
#pragma clang diagnostic ignored "-Wint-to-pointer-cast"

typedef __attribute__((ext_vector_type(4))) unsigned u32x4;
typedef __attribute__((ext_vector_type(2))) unsigned u32x2;
typedef __attribute__((ext_vector_type(4))) float f32x4;
typedef __attribute__((ext_vector_type(2))) float f32x2;
typedef __attribute__((ext_vector_type(8))) __bf16 bf16x8;
typedef __attribute__((ext_vector_type(2))) __bf16 bf16x2;
typedef __attribute__((ext_vector_type(4))) short s16x4;
typedef unsigned short u16;
typedef unsigned long long u64;
#define LAS __attribute__((address_space(3)))
#define GAS __attribute__((address_space(1)))
#define INL __device__ __forceinline__

constexpr int DM = 1024, NH = 32, DH = 128, SK = 8192, WIN = 2048, HD = NH * DH;
constexpr float SCALE = 0.088388347648318440f, LOG2E = 1.4426950408889634f;
constexpr int NWG = 256, NTHR = 512;

#define SCH 1750
#define SCH0 740
#define DLO 4
#define DHI 7
#define DSH 6
#define MAPO 1
#define MAPA 0
#define MAPB 0
#define PA_SLEEP 1
#define Y_SLEEP 0
#define TOUCH 1
#define LATER 16
#define TBASE 0
#define PW 5
#define ENDR 0
#define QA_SLEEP 8
#define NAP_Q 6
#define NAP_M 12
#define NAP_O 18

constexpr unsigned WS_EPOCH = 0;
constexpr unsigned WS_T0 = 4096;
constexpr unsigned WS_QA = WS_T0 + NWG * 16;
constexpr unsigned PA_REC = 1152;
constexpr unsigned WS_PA = WS_QA + NWG * 2048;
constexpr unsigned WS_YA = WS_PA + NWG * PA_REC;
constexpr unsigned WS_QB = WS_YA + NWG * 2048;
constexpr unsigned PB_REC = 4096 + 128;
constexpr unsigned WS_PB = WS_QB + 64 * 1024;
constexpr unsigned WS_OB = WS_PB + 128 * PB_REC;
constexpr unsigned WS_YB = WS_OB + 128 * 512;
constexpr unsigned WS_END = WS_YB + 64 * 2048;
static_assert(WS_END <= (4u << 20), "workspace");

constexpr unsigned L_R0 = 0, L_R1 = 32768, L_R2 = 65536, L_R3 = 98304;
constexpr unsigned L_QA = 131072;
constexpr unsigned L_OA = L_QA + 256;
constexpr unsigned L_PART = L_OA + 4 * 576;
constexpr unsigned L_XA = L_PART + 64 + 4 * 512;
constexpr unsigned L_VB = L_XA + 2048;
constexpr unsigned L_SC = L_VB + 4 * 2112;
constexpr unsigned L_CNT = L_SC + 8192;
constexpr unsigned LDS_BYTES = L_CNT + 64 + 256;
enum { C_WOA = 0, C_B, C_FREE0, C_FREE1, C_QA, C_VB, C_PB, C_AGG, C_AGG2, C_LATE, C_T0 };
constexpr unsigned SPIN_CAP = 1u << 17;

#define STAMP(i) do { } while (0)

struct Params {
  const u16 *wqA, *xA, *kA, *vA, *woA, *xB, *kB, *vB, *wqB, *woB;
  u16* out;
  unsigned char* ws;
};

template <int V> struct IC { static constexpr int value = V; };
template <int A, int B, typename F> INL void static_for(F&& f) { if constexpr (A < B) { f(IC<A>{}); static_for<A + 1, B>(f); } }
INL float bf2f(u16 v) { return __uint_as_float(((unsigned)v) << 16); }
INL u16 f2bf(float f) { return __builtin_bit_cast(u16, (__bf16)f); }
INL float bf16r(float f) { return bf2f(f2bf(f)); }
INL unsigned pack2(float lo, float hi) { return __builtin_bit_cast(unsigned, __builtin_convertvector(f32x2{lo, hi}, bf16x2)); }
INL float lo16(unsigned u) { return __uint_as_float(u << 16); }
INL float hi16(unsigned u) { return __uint_as_float(u & 0xffff0000u); }
template <int CTRL> INL float dppf(float x) { return __builtin_bit_cast(float, __builtin_amdgcn_mov_dpp(__builtin_bit_cast(int, x), CTRL, 0xf, 0xf, true)); }
template <int CTRL> INL unsigned dppu(unsigned x) { return (unsigned)__builtin_amdgcn_mov_dpp((int)x, CTRL, 0xf, 0xf, true); }
constexpr int XOR1 = 0xB1, XOR2 = 0x4E, ROR4 = 0x124, ROR8 = 0x128, BCAST = 0x150;
INL float x16_max(float x) { auto s = __builtin_amdgcn_permlane16_swap(__float_as_uint(x), __float_as_uint(x), false, false); return fmaxf(__uint_as_float(s[0]), __uint_as_float(s[1])); }
INL float x32_max(float x) { auto s = __builtin_amdgcn_permlane32_swap(__float_as_uint(x), __float_as_uint(x), false, false); return fmaxf(__uint_as_float(s[0]), __uint_as_float(s[1])); }
INL float x16_sum(float x) { auto s = __builtin_amdgcn_permlane16_swap(__float_as_uint(x), __float_as_uint(x), false, false); return __uint_as_float(s[0]) + __uint_as_float(s[1]); }
INL float x32_sum(float x) { auto s = __builtin_amdgcn_permlane32_swap(__float_as_uint(x), __float_as_uint(x), false, false); return __uint_as_float(s[0]) + __uint_as_float(s[1]); }
INL f32x4 mfma16(u32x4 a, u32x4 b, f32x4 c) { return __builtin_amdgcn_mfma_f32_16x16x32_bf16(__builtin_bit_cast(bf16x8, a), __builtin_bit_cast(bf16x8, b), c, 0, 0, 0); }
INL float ex2(float x) { return __builtin_amdgcn_exp2f(x); }
INL float mulL(float m) { float r = m * LOG2E; asm volatile("" : "+v"(r)); return r; }
INL u32x4 asu(const f32x4& v) { return __builtin_bit_cast(u32x4, v); }
INL f32x4 asf(const u32x4& v) { return __builtin_bit_cast(f32x4, v); }
INL float sel4(const f32x4& d, int g) { return (g & 2) ? ((g & 1) ? d[3] : d[2]) : ((g & 1) ? d[1] : d[0]); }
template <typename T> INL void pin(T& d) { asm volatile("" : "+v"(d)); }

INL u32x4 lds128(unsigned a) { return *(const LAS u32x4*)a; }
INL f32x2 lds64f(unsigned a) { return *(const LAS f32x2*)a; }
INL u32x2 lds_tr(unsigned a) { return __builtin_bit_cast(u32x2, __builtin_amdgcn_ds_read_tr16_b64_v4i16((LAS s16x4*)a)); }
INL void lds_signal(int c) {
  u64 keep;
  asm volatile("s_waitcnt lgkmcnt(0)\n\ts_mov_b64 %0, exec\n\ts_mov_b64 exec, 1\n\tds_add_u32 %1, %2\n\ts_mov_b64 exec, %0" : "=&s"(keep) : "v"(L_CNT + 4 * c), "v"(1u) : "memory");
}
INL unsigned lds_ticket(int c) {
  unsigned old; u64 keep;
  asm volatile("s_waitcnt lgkmcnt(0)\n\ts_mov_b64 %1, exec\n\ts_mov_b64 exec, 1\n\tds_add_rtn_u32 %0, %2, %3\n\ts_mov_b64 exec, %1\n\ts_waitcnt lgkmcnt(0)"
               : "=&v"(old), "=&s"(keep) : "v"(L_CNT + 4 * c), "v"(1u) : "memory");
  return __builtin_amdgcn_readfirstlane(old);
}
INL void lds_wait(int c, unsigned n) {
#pragma unroll 1
  for (unsigned s = 0; s < SPIN_CAP * 8; ++s) {
    if ((unsigned)__builtin_amdgcn_readfirstlane(*(volatile LAS unsigned*)(L_CNT + 4 * c)) >= n) break;
    __builtin_amdgcn_s_sleep(1);
  }
  asm volatile("" ::: "memory");
}
INL void wg_barrier() { asm volatile("s_waitcnt lgkmcnt(0)\n\ts_barrier" ::: "memory"); }
INL void nap(int n) {
#pragma unroll 1
  for (int i = 0; i < n; ++i) __builtin_amdgcn_s_sleep(64);
}

template <int OFF> INL void aload(u32x4& d, unsigned voff, const void* sbase) {
  asm volatile("global_load_dwordx4 %0, %1, %2 offset:%3 nt" : "+v"(d) : "v"(voff), "s"(sbase), "i"(OFF) : "memory");
}
template <int N> INL void waitv(u32x4& d) { asm volatile("s_waitcnt vmcnt(%1)" : "+v"(d) : "i"(N) : "memory"); }
template <int N> INL void waitvm() { asm volatile("s_waitcnt vmcnt(%0)" :: "i"(N) : "memory"); }
INL void pacev(u32x4& d, unsigned depth) {
  asm volatile("s_cmp_eq_u32 %1, 7\n\ts_cbranch_scc1 7f\n\ts_cmp_eq_u32 %1, 6\n\ts_cbranch_scc1 6f\n\ts_cmp_eq_u32 %1, 5\n\ts_cbranch_scc1 5f\n\t"
               "s_waitcnt vmcnt(4)\n5:\n\ts_waitcnt vmcnt(5)\n6:\n\ts_waitcnt vmcnt(6)\n7:\n\ts_waitcnt vmcnt(7)" : "+v"(d) : "s"(depth) : "memory", "scc");
}
template <int W, int OFF> INL void stage(u32x4& d, unsigned lds_addr) {
  asm volatile("s_waitcnt vmcnt(%2)\n\tds_write_b128 %1, %0 offset:%3" : "+v"(d) : "v"(lds_addr), "i"(W), "i"(OFF) : "memory");
}

INL float dec(unsigned v) { return __uint_as_float(v); }
INL void put2(const unsigned char* ws, unsigned off, float a, float b, unsigned tag, u64 mask) {
  u64 keep;
  asm volatile("s_mov_b64 %0, exec\n\ts_mov_b64 exec, %4\n\tglobal_store_dwordx4 %1, %2, %3 sc1\n\ts_mov_b64 exec, %0\n\ts_nop 0"
               : "=&s"(keep) : "v"(off), "v"(u32x4{__float_as_uint(a), tag, __float_as_uint(b), tag}), "s"(ws), "s"(mask) : "memory");
}
INL void ld16m(u32x4& d, const unsigned char* ws, unsigned off, u64 mask) {
  asm volatile("s_mov_b64 exec, %3\n\tglobal_load_dwordx4 %0, %1, %2 sc1\n\ts_mov_b64 exec, -1" : "+v"(d) : "v"(off), "s"(ws), "s"(mask) : "memory");
}
template <int N, int SLEEP> INL void gather(u32x4 (&v)[N], const unsigned char* ws, const unsigned (&off)[N], unsigned tag) {
  u64 need[N];
#pragma unroll
  for (int i = 0; i < N; ++i) { asm volatile("" : "=v"(v[i])); need[i] = ~0ull; }
#pragma unroll 1
  for (unsigned s = 0; s < SPIN_CAP; ++s) {
#pragma unroll
    for (int i = 0; i < N; ++i) ld16m(v[i], ws, off[i], need[i]);
    waitvm<0>();
    unsigned bad[N], any = 0;
#pragma unroll
    for (int i = 0; i < N; ++i) pin(v[i]);
#pragma unroll
    for (int i = 0; i < N; ++i) { bad[i] = (v[i][1] ^ tag) | (v[i][3] ^ tag); any |= bad[i]; }
    pin(any);
    if (__builtin_amdgcn_ballot_w64(any != 0) == 0) break;
#pragma unroll
    for (int i = 0; i < N; ++i) {
      const u64 m = __builtin_amdgcn_ballot_w64(bad[i] != 0);
      asm volatile("s_cmp_lg_u64 %1, 0\n\ts_cselect_b64 %0, -1, 0" : "=s"(need[i]) : "s"(m) : "scc");
    }
    if (SLEEP) __builtin_amdgcn_s_sleep(SLEEP);
  }
}
INL void publish256(const unsigned char* ws, unsigned off, int cnt, int lane, unsigned tag) {
  if (lds_ticket(cnt) == 3) {
    const f32x2 e0 = lds64f(L_OA + lane * 8), e1 = lds64f(L_OA + 512 + lane * 8);
    put2(ws, off + lane * 16, e0[0], e0[1], tag, ~0ull);
    put2(ws, off + 1024 + lane * 16, e1[0], e1[1], tag, ~0ull);
  }
}
INL void store_bf16(u16* out, unsigned byte_off, float v) { asm volatile("global_store_short %0, %1, %2 sc1" :: "v"(byte_off), "v"(pack2(v, v)), "s"(out) : "memory"); }

INL unsigned score_step(u32x4 (&R)[8], const u32x4 (&QB)[4], unsigned voff, const char* next, unsigned depth) {
  f32x4 s[2] = {{0.f, 0.f, 0.f, 0.f}, {0.f, 0.f, 0.f, 0.f}};
  static_for<0, 8>([&](auto ic) __attribute__((always_inline)) {
    constexpr int i = decltype(ic)::value;
    pacev(R[i], depth);
    s[i >> 2] = mfma16(R[i], QB[i & 3], s[i >> 2]);
    __builtin_amdgcn_sched_barrier(0);
    aload<1024 * i - 4096>(R[i], voff, next);
    __builtin_amdgcn_sched_barrier(0);
  });
  float d = dppf<0x00>(s[0][0]); d += dppf<0x55>(s[0][1]); d += dppf<0xAA>(s[0][2]); d += dppf<0xFF>(s[0][3]);
  float e = dppf<0x00>(s[1][0]); e += dppf<0x55>(s[1][1]); e += dppf<0xAA>(s[1][2]); e += dppf<0xFF>(s[1][3]);
  const unsigned u = pack2(d, e);
  return pack2(lo16(u) * SCALE, hi16(u) * SCALE);
}
template <bool MORE>
INL void value_step(f32x4& acc, float& l, float mL, unsigned sc, u32x4 (&R)[8], const unsigned (&msk)[4], unsigned voff, const char* next, unsigned depth) {
  const float pa = ex2(__builtin_fmaf(lo16(sc), LOG2E, -mL)), pb = ex2(__builtin_fmaf(hi16(sc), LOG2E, -mL));
  l += pa + pb;
  unsigned pk[2] = {pack2(pa, pa), pack2(pb, pb)};
  pin(pk[0]); pin(pk[1]);
  static_for<0, 8>([&](auto ic) __attribute__((always_inline)) {
    constexpr int i = decltype(ic)::value;
    const unsigned b = pk[i >> 2];
    const u32x4 P = {dppu<BCAST + 4 * (i & 3)>(b) & msk[0], dppu<BCAST + 4 * (i & 3)>(b) & msk[1], dppu<BCAST + 4 * (i & 3)>(b) & msk[2], dppu<BCAST + 4 * (i & 3)>(b) & msk[3]};
    if constexpr (MORE) pacev(R[i], depth); else waitv<7 - i>(R[i]);
    acc = mfma16(R[i], P, acc);
    if constexpr (MORE) { __builtin_amdgcn_sched_barrier(0); aload<1024 * i - 4096>(R[i], voff, next); }
  });
}
INL unsigned pace_depth(unsigned t0, unsigned round) {
  const int late = (int)((unsigned)__builtin_amdgcn_s_memrealtime() - t0 - (SCH0 + ((round * SCH) >> 4)));
  if (ENDR && round > 64 - ENDR) return 7;
  return __builtin_amdgcn_readfirstlane(min(max(((late >> DSH) + 1) + DLO, DLO), DHI));
}

INL void load_wfrag(u32x4 (&B)[8], unsigned region, int w, int lane) {
  const int g = lane >> 4, q = (lane >> 2) & 3, pp = lane & 3;
  const unsigned sw = 2u * ((q >> 1) | ((g & 1) << 1));
  const unsigned a = region + (8 * g + q) * 128 + 16 * ((2 * w + (pp >> 1)) ^ sw) + 8 * (pp & 1);
#pragma unroll
  for (int kk = 0; kk < 8; ++kk) { const u32x2 lo = lds_tr(a + kk * 4096), hi = lds_tr(a + kk * 4096 + 512); B[kk] = u32x4{lo[0], lo[1], hi[0], hi[1]}; }
}
INL f32x4 piece(unsigned a, const u32x4 (&B)[8], f32x4 d) {
  u32x4 A[8];
#pragma unroll
  for (int kk = 0; kk < 8; ++kk) A[kk] = lds128(a + 64 * kk);
  __builtin_amdgcn_sched_barrier(0);
#pragma unroll
  for (int kk = 0; kk < 8; ++kk) d = mfma16(A[kk], B[kk], d);
  return d;
}
INL f32x4 piece4(int lane, int w) {
  f32x4 d = {0.f, 0.f, 0.f, 0.f};
#pragma unroll 1
  for (int tt = 0; tt < 4; ++tt) { u32x4 B[8]; load_wfrag(B, ((tt + 2) & 3) * 32768, w, lane); d = piece(L_VB + tt * 512 + (lane & 3) * 2112 + 16 * (lane >> 4), B, d); }
  return d;
}
INL unsigned wtile_voff(int lane, int odd, unsigned rs) {
  const unsigned sw = 2u * (((lane >> 4) & 1) | (odd << 1));
  return (lane >> 3) * rs + 16 * ((lane & 7) ^ sw);
}

__global__ void __launch_bounds__(NTHR, 2) fused_decode(Params p) {
  const int lane = threadIdx.x & 63, wave = __builtin_amdgcn_readfirstlane(threadIdx.x >> 6);
  const int j = blockIdx.x, g = lane >> 4, c = lane & 15;
  const int jo = ((j & 15) << 4) | (j >> 4);
  const int ja = j;
  const int jq = j ^ 8, qd = j >> 2, job = (j ^ (j >> 3)) & 3, idx = 2 * qd + (job & 1);

  if (wave >= 4) {
    const int w = wave - 4;
    const unsigned th = (unsigned)__builtin_amdgcn_s_memrealtime();
    u32x4 xv = {0u, 0u, 0u, 0u};
    if (w < 2) asm volatile("global_load_dwordx4 %0, %1, %2" : "+v"(xv) : "v"((lane >> 5) * DM * 2 + (lane & 31) * 16), "s"((const char*)p.xA + (w * 2 * DM + (j & 3) * 256) * 2) : "memory");
    if (w == 3 && lane < 16) *(LAS unsigned*)(L_CNT + 4 * lane) = 0u;
    u64 epoch;
    asm volatile("s_load_dwordx2 %0, %1, 0x0\n\ts_waitcnt lgkmcnt(0)" : "=s"(epoch) : "s"(p.ws + WS_EPOCH) : "memory");
    u64 z = epoch + 0x9E3779B97F4A7C15ull;
    z = (z ^ (z >> 30)) * 0xBF58476D1CE4E5B9ull; z = (z ^ (z >> 27)) * 0x94D049BB133111EBull; z ^= z >> 31;
    const unsigned tag = (unsigned)(z >> 32);
    if (TBASE != 0 && w == 2) put2(p.ws, WS_T0 + jq * 16, dec(th), dec(th), tag, 1ull);
    if (w < 2) { waitv<0>(xv); *(LAS u32x4*)(L_XA + w * 1024 + lane * 16) = xv; }
    wg_barrier();
    if (w == 0) STAMP(3);
    u32x4 F[8];
    load_wfrag(F, L_R0, w, lane);
    *(LAS float*)(L_OA + (g * 64 + 16 * w + c) * 4) = sel4(piece(L_XA + (lane & 3) * 512 + 16 * g, F, f32x4{0.f, 0.f, 0.f, 0.f}), g);
    publish256(p.ws, WS_QA + jq * 2048, C_AGG, lane, tag);
    lds_signal(C_FREE0);
    lds_wait(C_WOA, 4);
    load_wfrag(F, L_R1, w, lane);
#pragma unroll
    for (int kk = 0; kk < 8; ++kk) pin(F[kk]);
    lds_signal(C_FREE1);
    if (w == 0) {
      constexpr int NQ = TBASE != 0 ? 5 : 4;
      u32x4 v[NQ]; unsigned off[NQ];
#pragma unroll
      for (int i = 0; i < 4; ++i) off[i] = WS_QA + (8 * ((ja >> 1) & 31) + 4 * (lane >> 5) + i) * 2048 + (ja >> 6) * 512 + (lane & 31) * 16;
      if (TBASE != 0) off[NQ - 1] = WS_T0 + (8 * ((ja >> 1) & 31) + (lane & 7)) * 16;
      gather<NQ, QA_SLEEP>(v, p.ws, off, tag);
      if (TBASE != 0) {
        int dt = (int)(v[NQ - 1][0] - th);
        if (TBASE == 1) { dt += (int)dppu<XOR1>(dt); dt += (int)dppu<XOR2>(dt); dt += (int)dppu<0x141>(dt); dt >>= 3; }
        else { dt = min(dt, (int)dppu<XOR1>(dt)); dt = min(dt, (int)dppu<XOR2>(dt)); dt = min(dt, (int)dppu<0x141>(dt)); }
        *(LAS unsigned*)(L_CNT + 4 * C_T0) = th + dt;
      }
      *(LAS unsigned*)(L_QA + 4 * lane) = pack2((dec(v[0][0]) + dec(v[1][0])) + (dec(v[2][0]) + dec(v[3][0])), (dec(v[0][2]) + dec(v[1][2])) + (dec(v[2][2]) + dec(v[3][2])));
      lds_signal(C_QA);
      STAMP(10);
    }
    if (job >= 2) {
      {
        nap(NAP_Q);
        u32x4 v[1]; const unsigned off[1] = {WS_QB + (((idx >> 3) & 3) * 16 + 4 * w + g) * 1024 + (idx >> 5) * 256 + c * 16};
        gather<1, 64>(v, p.ws, off, tag);
        *(LAS u32x2*)(L_VB + (2 * w + (lane >> 5)) * 272 + (lane & 31) * 8) = u32x2{v[0][0], v[0][2]};
        lds_signal(C_VB);
      }
      lds_wait(C_VB, 4);
      lds_wait(C_B, 4);
      u32x4 Q[4];
#pragma unroll
      for (int kk = 0; kk < 4; ++kk) Q[kk] = lds128(L_VB + (c & 7) * 272 + 64 * kk + 16 * g);
      float s[16][4]; float mx = -INFINITY;
#pragma unroll
      for (int blk = 0; blk < 16; ++blk) {
        f32x4 d = {0.f, 0.f, 0.f, 0.f};
#pragma unroll
        for (int kk = 0; kk < 4; ++kk) d = mfma16(lds128(L_R2 + (16 * blk + c) * 256 + 16 * ((4 * kk + g) ^ ((c & 3) << 2))), Q[kk], d);
#pragma unroll
        for (int r = 0; r < 4; ++r) { s[blk][r] = bf16r(bf16r(d[r]) * SCALE); mx = fmaxf(mx, s[blk][r]); }
      }
      const float mL = mulL(x32_max(x16_max(mx)));
      float l = 0.f;
#pragma unroll
      for (int blk = 0; blk < 16; ++blk)
#pragma unroll
        for (int r = 0; r < 4; ++r) { s[blk][r] = ex2(__builtin_fmaf(s[blk][r], LOG2E, -mL)); l += s[blk][r]; }
      l = x32_sum(x16_sum(l));
      f32x4 o0 = {0.f, 0.f, 0.f, 0.f}, o1 = {0.f, 0.f, 0.f, 0.f};
      const int q = (lane >> 2) & 3, pp = lane & 3;
      const unsigned va0 = L_R0 + (4 * g + q) * 256 + 16 * ((4 * w + (pp >> 1)) ^ (q << 2)) + 8 * (pp & 1);
#pragma unroll
      for (int tt = 0; tt < 8; ++tt) {
        const u32x4 P = {pack2(s[2 * tt][0], s[2 * tt][1]), pack2(s[2 * tt][2], s[2 * tt][3]), pack2(s[2 * tt + 1][0], s[2 * tt + 1][1]), pack2(s[2 * tt + 1][2], s[2 * tt + 1][3])};
        const u32x2 a0 = lds_tr(va0 + tt * 8192), a1 = lds_tr(va0 + tt * 8192 + 4096), b0 = lds_tr(va0 + tt * 8192 + 32), b1 = lds_tr(va0 + tt * 8192 + 4096 + 32);
        o0 = mfma16(u32x4{a0[0], a0[1], a1[0], a1[1]}, P, o0);
        o1 = mfma16(u32x4{b0[0], b0[1], b1[0], b1[1]}, P, o1);
      }
      asm volatile("global_store_dwordx4 %0, %1, %2 sc1 nt\n\ts_waitcnt vmcnt(0)" :: "v"(WS_PB + idx * PB_REC + w * 1024 + (c & 7) * 128 + (c >> 3) * 64 + g * 16), "v"(asu(c < 8 ? o0 : o1)), "s"(p.ws) : "memory");
      if (lds_ticket(C_PB) == 3) put2(p.ws, WS_PB + idx * PB_REC + 4096 + (lane & 7) * 16, mL, l, tag, 0xffull);
    } else {
      if (job == 0) {
        u32x4 v[2] = {{0u, 0u, 0u, 0u}, {0u, 0u, 0u, 0u}};
        asm volatile("global_load_dwordx4 %0, %2, %3\n\tglobal_load_dwordx4 %1, %2, %3 offset:1024\n\ts_waitcnt vmcnt(0)" : "+v"(v[0]), "+v"(v[1]) : "v"(lane * 16), "s"(p.xB + w * DM) : "memory");
        *(LAS u32x4*)(L_VB + w * 2112 + lane * 16) = v[0]; *(LAS u32x4*)(L_VB + w * 2112 + 1024 + lane * 16) = v[1];
        lds_signal(C_VB);
        lds_wait(C_VB, 4);
        lds_wait(C_B, 4);
        *(LAS u16*)(L_OA + (g * 64 + 16 * w + c) * 2) = f2bf(sel4(piece4(lane, w), g));
        if (lds_ticket(C_AGG2) == 3) { const f32x2 e = lds64f(L_OA + lane * 8); put2(p.ws, WS_QB + qd * 1024 + lane * 16, e[0], e[1], tag, ~0ull); }
      }
      if (w == 3) {
        nap(NAP_M);
        const unsigned rec = WS_PB + ((idx & ~7) + (lane >> 5)) * PB_REC;
        u32x4 ml[1]; const unsigned off[1] = {rec + 2 * (lane & 3) * PB_REC + 4096 + (idx & 7) * 16};
        gather<1, 64>(ml, p.ws, off, tag);
        u32x4 v[4];
        const unsigned po = rec + ((lane >> 3) & 3) * 1024 + (idx & 7) * 128 + (lane & 7) * 16;
        asm volatile("global_load_dwordx4 %0, %4, %8 sc1 nt\n\tglobal_load_dwordx4 %1, %5, %8 sc1 nt\n\tglobal_load_dwordx4 %2, %6, %8 sc1 nt\n\tglobal_load_dwordx4 %3, %7, %8 sc1 nt\n\ts_waitcnt vmcnt(0)"
                     : "=&v"(v[0]), "=&v"(v[1]), "=&v"(v[2]), "=&v"(v[3]) : "v"(po), "v"(po + 2 * PB_REC), "v"(po + 4 * PB_REC), "v"(po + 6 * PB_REC), "s"(p.ws) : "memory");
        const float m = dec(ml[0][0]);
        float M = fmaxf(m, dppf<XOR1>(m)); M = fmaxf(M, dppf<XOR2>(M)); M = x32_max(M);
        const float wt = ex2(m - M);
        float L = wt * dec(ml[0][2]); L += dppf<XOR1>(L); L += dppf<XOR2>(L); L = x32_sum(L);
        f32x4 a = asf(v[0]) * dppf<0x00>(wt) + asf(v[1]) * dppf<0x55>(wt) + asf(v[2]) * dppf<0xAA>(wt) + asf(v[3]) * dppf<0xFF>(wt);
#pragma unroll
        for (int r = 0; r < 4; ++r) a[r] = x32_sum(a[r]) / L;
        put2(p.ws, WS_OB + ((((idx >> 3) & 3) * 4 + (idx >> 5)) * 8 + (idx & 7)) * 512 + (lane & 31) * 16, dec(pack2(a[0], a[1])), dec(pack2(a[2], a[3])), tag, 0xffffffffull);
      }
      if (job == 1) {
        nap(w == 3 ? NAP_O - NAP_M : NAP_O);
        u32x4 v[4]; unsigned off[4];
#pragma unroll
        for (int i = 0; i < 4; ++i) off[i] = WS_OB + ((qd >> 4) * 4 + w) * 4096 + i * 1024 + lane * 16;
        gather<4, 64>(v, p.ws, off, tag);
#pragma unroll
        for (int i = 0; i < 4; ++i) *(LAS u32x2*)(L_VB + w * 2112 + i * 512 + lane * 8) = u32x2{v[i][0], v[i][2]};
        lds_signal(C_VB);
        lds_wait(C_VB, 4);
        lds_wait(C_B, 4);
        const f32x4 d = piece4(lane, w);
        if (g < 2) *(LAS f32x2*)(L_OA + w * 256 + (lane & 31) * 8) = g ? f32x2{d[2], d[3]} : f32x2{d[0], d[1]};
        publish256(p.ws, WS_YB + qd * 2048, C_AGG2, lane, tag);
        if (w == 0) {
          nap(2);
          u32x4 y[2];
          const unsigned base = WS_YB + ((lane >> 5) * 16 + (qd & 15)) * 2048 + (qd >> 4) * 512 + (lane & 31) * 16, yo[2] = {base, base + 32 * 2048};
          gather<2, 64>(y, p.ws, yo, tag);
          const float a = x32_sum(dec(y[0][0]) + dec(y[1][0])), b = x32_sum(dec(y[0][2]) + dec(y[1][2]));
          store_bf16(p.out, ((2 * (g & 1) + (g >> 1)) * 2 * DM + DM + 64 * (qd & 15) + 16 * (qd >> 4) + c) * 2, g < 2 ? a : b);
        }
      }
    }
    if (w == 1) STAMP(5);
    unsigned off[8], yo[2], pa = WS_PA + (8 * ((ja >> 1) & 31) + 2 * (ja >> 6) + (ja & 1)) * PA_REC + (w ? 1024 + (lane & 7) * 16 : lane * 16);
#pragma unroll
    for (int i = 0; i < 4; ++i) { const unsigned rec = WS_PA + ((jo & ~15) + 8 * (i >> 1) + 2 * w + (i & 1)) * PA_REC; off[i] = rec + lane * 16; off[4 + i] = rec + 1024 + (lane & 7) * 16; pin(off[i]); pin(off[4 + i]); }
    const int n = 64 * (jo & 15) + 16 * w + c;
    unsigned ya = WS_YA + ((g & 1) * 128 + (n >> 3)) * 2048 + (jo >> 4) * 128 + (n & 7) * 16;
    unsigned oa = ((2 * (j >> 7) + ((lane >> 3) & 1)) * 2 * DM + 8 * (j & 127) + (lane & 7)) * 2;
    unsigned lp = L_PART + 64 + 8 * lane, lw = L_OA + w * 576 + 4 * lane, lr = L_OA + (lane & 3) * 576 + 16 * g;
    yo[0] = WS_YA + j * 2048 + lane * 16; yo[1] = yo[0] + 1024;
    pin(pa); pin(ya); pin(oa); pin(lp); pin(lw); pin(lr); pin(yo[0]); pin(yo[1]);
    if (TOUCH != 0) {
      if (TOUCH == 2) lds_wait(C_LATE, 1);
      u32x4 dum;
      asm volatile("global_load_dwordx4 %0, %1, %2 sc1\n\ts_waitcnt vmcnt(0)" : "=&v"(dum) : "v"(w < 2 ? pa : yo[w & 1]), "s"(p.ws) : "memory");
    }
    waitvm<0>();
    wg_barrier();
    if (w < 2) {
      if (w == 0) STAMP(6);
      const f32x4 m01 = asf(lds128(L_PART)), m23 = asf(lds128(L_PART + 16));
      f32x2 oo[4];
#pragma unroll
      for (int i = 0; i < 4; ++i) oo[i] = lds64f(lp + i * 512);
      __builtin_amdgcn_sched_barrier(0);
      const float M = fmaxf(fmaxf(m01[0], m01[2]), fmaxf(m23[0], m23[2]));
      const float w0 = ex2(m01[0] - M), w1 = ex2(m01[2] - M), w2 = ex2(m23[0] - M), w3 = ex2(m23[2] - M);
#pragma unroll
      for (int i = 0; i < 4; ++i) pin(oo[i]);
      if (w == 0) put2(p.ws, pa, (w0 * oo[0][0] + w1 * oo[1][0]) + (w2 * oo[2][0] + w3 * oo[3][0]), (w0 * oo[0][1] + w1 * oo[1][1]) + (w2 * oo[2][1] + w3 * oo[3][1]), tag, ~0ull);
      else put2(p.ws, pa, M, (w0 * m01[1] + w1 * m01[3]) + (w2 * m23[1] + w3 * m23[3]), tag, 0xffull);
    }
    u32x4 v[8];
    gather<8, PA_SLEEP>(v, p.ws, off, tag);
#pragma unroll
    for (int hh = 0; hh < 2; ++hh) {
      const float m0 = dec(v[4 + 2 * hh][0]), m1 = dec(v[5 + 2 * hh][0]), M = fmaxf(m0, m1);
      const float w0 = ex2(m0 - M), w1 = ex2(m1 - M);
      const float rl = __builtin_amdgcn_rcpf(w0 * dec(v[4 + 2 * hh][2]) + w1 * dec(v[5 + 2 * hh][2]));
      const float a0 = w0 * dec(v[2 * hh][0]) + w1 * dec(v[2 * hh + 1][0]), a1 = w0 * dec(v[2 * hh][2]) + w1 * dec(v[2 * hh + 1][2]);
      *(LAS unsigned*)(lw + 256 * hh) = pack2(a0 * rl, a1 * rl);
    }
    wg_barrier();
    const f32x4 d = piece(lr, F, f32x4{0.f, 0.f, 0.f, 0.f});
    put2(p.ws, ya, g ? d[2] : d[0], g ? d[3] : d[1], tag, 0xffffffffull);
    if (w == 0) {
      STAMP(8);
      u32x4 y[2];
      gather<2, Y_SLEEP>(y, p.ws, yo, tag);
      const float a = dec(y[0][0]) + dec(y[1][0]), b = dec(y[0][2]) + dec(y[1][2]);
      const float k = (lane & 8) ? b : a, sd = (lane & 8) ? a : b;
      const float r = x32_sum(x16_sum(k + dppf<ROR8>(sd)));
      if (lane < 16) store_bf16(p.out, oa, r);
      STAMP(9);
      if (j == 0 && lane == 0) __hip_atomic_store((GAS u64*)(p.ws + WS_EPOCH), epoch + 1, __ATOMIC_RELAXED, __HIP_MEMORY_SCOPE_AGENT);
    }
    return;
  }

  if (wave == 0) STAMP(0);
  const unsigned vq0 = wtile_voff(lane, 0, HD * 2), vq1 = wtile_voff(lane, 1, HD * 2);
  const size_t qoff = ((size_t)((j & 3) * 256 + wave * 64) * HD + (jq >> 2) * 64) * 2;
  u32x4 R[8];
  static_for<0, 8>([&](auto ic) __attribute__((always_inline)) {
    constexpr int i = decltype(ic)::value;
    u32x4& r = R[i];
    asm volatile("" : "=v"(r));
    aload<0>(r, (i & 1) ? vq1 : vq0, (const char*)p.wqA + qoff + (size_t)i * 8 * HD * 2);
  });
  unsigned t0 = (unsigned)__builtin_amdgcn_s_memrealtime();
  int jb = job; asm volatile("" : "+s"(jb));
  const unsigned vo0 = wtile_voff(lane, 0, DM * 2), vo1 = wtile_voff(lane, 1, DM * 2), vkv = g * 256 + 16 * (c ^ (g << 2));
  const size_t ooff = ((size_t)((jo >> 4) * 256 + wave * 64) * DM + (jo & 15) * 64) * 2;
  const unsigned vb0 = jb == 0 ? vq0 : jb == 1 ? vo0 : vkv, vb1 = jb == 0 ? vq1 : jb == 1 ? vo1 : vkv;
  unsigned bstep = jb == 0 ? 8 * HD * 2 : jb == 1 ? 8 * DM * 2 : 1024;
  asm volatile("" : "+s"(bstep));
  const unsigned boff = (jb == 0 ? qd * 128 : jb == 1 ? ((qd >> 4) * 1024 * DM + (qd & 15) * 64) * 2
                                                      : ((idx >> 3) * SK + (SK - WIN) + (idx & 7) * 256) * DH * 2) + wave * 8 * bstep;
  const char* bp = (const char*)(jb == 0 ? p.wqB : jb == 1 ? p.woB : p.kB) + boff;
  const char* const b23 = jb < 2 ? bp + 64 * bstep : (const char*)p.vB + boff;
  const unsigned voffV = lane * 16, voffK = ((lane >> 2) & 3) * 256 + g * 64 + (lane & 3) * 16;
  const unsigned ldst = wave * 8192 + lane * 16;
  const char* const kp = (const char*)p.kA + (size_t)ja * 1048576 + wave * 8192 + 4096;
  const char* const vp = (const char*)p.vA + (size_t)ja * 1048576 + wave * 8192 + 4096;
  static_for<8, 56>([&](auto nc) __attribute__((always_inline)) {
    constexpr int n = decltype(nc)::value, k = n >> 3, i = n & 7;
    constexpr unsigned reg = ((k - 1) & 3) * 32768;
    if constexpr (n == 40) lds_wait(C_FREE0, 4);
    if constexpr (n == 48) lds_wait(C_FREE1, 4);
    stage<n < 16 ? 7 : PW, (reg & 0xffffu) + i * 1024>(R[i], ldst + (reg & ~0xffffu));
    if constexpr (k == 1) aload<0>(R[i], (i & 1) ? vo1 : vo0, (const char*)p.woA + ooff + (size_t)i * 8 * DM * 2);
    if constexpr (k >= 2 && k <= 5) {
      if constexpr (n == 32) bp = b23;
      aload<0>(R[i], (i & 1) ? vb1 : vb0, bp);
      bp += (i == 7 ? 25 : 1) * bstep;
    }
    if constexpr (k == 6) aload<1024 * i - 4096>(R[i], voffK, kp);
    if constexpr (n == 15) wg_barrier();
    if constexpr (n == 23) lds_signal(C_WOA);
    if constexpr (n == 55) lds_signal(C_B);
  });
  if (wave == 0) STAMP(1);
  lds_wait(C_QA, 1);
  if (wave == 0) STAMP(2);
  if (TBASE != 0) t0 = __builtin_amdgcn_readfirstlane(*(const LAS unsigned*)(L_CNT + 4 * C_T0));
  const unsigned sca = L_SC + wave * 2048 + (lane >> 2) * 4;
  float mx = -INFINITY;
  {
    u32x4 QB[4];
    const u32x4 qv = lds128(L_QA + 16 * (4 * g + (lane & 3)));
#pragma unroll
    for (int i = 0; i < 4; ++i) QB[i] = ((c >> 2) == i) ? qv : u32x4{0u, 0u, 0u, 0u};
#pragma unroll 1
    for (unsigned r = 1; r <= 32; ++r) {
      const unsigned sc = score_step(R, QB, r == 32 ? voffV : voffK, r == 32 ? vp : kp + r * 32768, pace_depth(t0, r));
      *(LAS unsigned*)(sca + (r - 1) * 64) = sc;
      mx = fmaxf(mx, fmaxf(lo16(sc), hi16(sc)));
    }
  }
  mx = fmaxf(mx, dppf<ROR4>(mx)); mx = fmaxf(mx, dppf<ROR8>(mx));
  const float mL = mulL(x32_max(x16_max(mx)));
  if (wave == 0) STAMP(4);
  unsigned msk[4];
#pragma unroll
  for (int i = 0; i < 4; ++i) msk[i] = (c == 2 * i) ? 0x0000ffffu : (c == 2 * i + 1) ? 0xffff0000u : 0u;
  f32x4 acc = {0.f, 0.f, 0.f, 0.f}; float l = 0.f;
#pragma unroll 1
  for (unsigned r = 1; r < 32; ++r) {
    if (TOUCH == 2 && r == LATER && wave == 0) *(volatile LAS unsigned*)(L_CNT + 4 * C_LATE) = 1u;
    value_step<true>(acc, l, mL, *(const LAS unsigned*)(sca + (r - 1) * 64), R, msk, voffV, vp + r * 32768, pace_depth(t0, 32 + r));
  }
  value_step<false>(acc, l, mL, *(const LAS unsigned*)(sca + 31 * 64), R, msk, 0u, nullptr, 0u);
  l += dppf<ROR4>(l); l += dppf<ROR8>(l); l = x32_sum(x16_sum(l));
  if (c < 8) {
#pragma unroll
    for (int r = 0; r < 4; ++r) *(LAS float*)(L_PART + 64 + wave * 512 + (8 * (4 * g + r) + c) * 4) = acc[r];
  }
  if (lane == 0) *(LAS f32x2*)(L_PART + wave * 8) = f32x2{mL, l};
  wg_barrier();
}

extern "C" void kernel_launch(void* const* d_in, const int* in_sizes, int n_in, void* d_out, int out_size, void* d_ws, size_t ws_size, hipStream_t stream) {
  Params p;
  p.xA = (const u16*)d_in[0]; p.kA = (const u16*)d_in[1]; p.vA = (const u16*)d_in[2]; p.wqA = (const u16*)d_in[3]; p.woA = (const u16*)d_in[4];
  p.xB = (const u16*)d_in[5]; p.kB = (const u16*)d_in[6]; p.vB = (const u16*)d_in[7]; p.wqB = (const u16*)d_in[8]; p.woB = (const u16*)d_in[9];
  p.out = (u16*)d_out; p.ws = (unsigned char*)d_ws;
  (void)hipFuncSetAttribute((const void*)fused_decode, hipFuncAttributeMaxDynamicSharedMemorySize, LDS_BYTES);
  fused_decode<<<NWG, NTHR, LDS_BYTES, stream>>>(p);
}
```

```cpp
#define PROBE 3
#define SCH0 810
#include <hip/hip_runtime.h>
#include <hip/hip_bf16.h>
#include <cstdint>
#pragma clang diagnostic ignored "-Wint-to-pointer-cast"

typedef __attribute__((ext_vector_type(4))) unsigned u32x4;
typedef __attribute__((ext_vector_type(2))) unsigned u32x2;
typedef __attribute__((ext_vector_type(4))) float f32x4;
typedef __attribute__((ext_vector_type(2))) float f32x2;
typedef __attribute__((ext_vector_type(8))) __bf16 bf16x8;
typedef __attribute__((ext_vector_type(2))) __bf16 bf16x2;
typedef __attribute__((ext_vector_type(4))) short s16x4;
typedef unsigned short u16;
typedef unsigned long long u64;
#define LAS __attribute__((address_space(3)))
#define GAS __attribute__((address_space(1)))
#define INL __device__ __forceinline__

constexpr int DM = 1024, NH = 32, DH = 128, SK = 8192, WIN = 2048, HD = NH * DH;
constexpr float SCALE = 0.088388347648318440f, LOG2E = 1.4426950408889634f;
constexpr int NWG = 256, NTHR = 512;

#define SCH 1765
#define DLO 4
#define DHI 7
#define DSH 6
#define MAPO 1
#define MAPA 0
#define MAPB 0
#define PA_SLEEP 1
#define Y_SLEEP 0
#define TOUCH 1
#define LATER 16
#define TBASE 1
#define SCHV 1715
#define JUMP 100
#define KLIN 0
#define PW 5
#define ENDR 0
#define QA_SLEEP 8
#define NAP_Q 6
#define NAP_M 12
#define NAP_O 18

constexpr unsigned WS_EPOCH = 0;
constexpr unsigned WS_T0 = 4096;
constexpr unsigned WS_QA = WS_T0 + NWG * 16;
constexpr unsigned PA_REC = 1152;
constexpr unsigned WS_PA = WS_QA + NWG * 2048;
constexpr unsigned WS_YA = WS_PA + NWG * PA_REC;
constexpr unsigned WS_QB = WS_YA + NWG * 2048;
constexpr unsigned PB_REC = 4096 + 128;
constexpr unsigned WS_PB = WS_QB + 64 * 1024;
constexpr unsigned WS_OB = WS_PB + 128 * PB_REC;
constexpr unsigned WS_YB = WS_OB + 128 * 512;
constexpr unsigned WS_END = WS_YB + 64 * 2048;
static_assert(WS_END <= (4u << 20), "workspace");

constexpr unsigned L_R0 = 0, L_R1 = 32768, L_R2 = 65536, L_R3 = 98304;
constexpr unsigned L_QA = 131072;
constexpr unsigned L_OA = L_QA + 256;
constexpr unsigned L_PART = L_OA + 4 * 576;
constexpr unsigned L_XA = L_PART + 64 + 4 * 512;
constexpr unsigned L_VB = L_XA + 2048;
constexpr unsigned L_SC = L_VB + 4 * 2112;
constexpr unsigned L_CNT = L_SC + 8192;
constexpr unsigned LDS_BYTES = L_CNT + 64 + 256;
enum { C_WOA = 0, C_B, C_FREE0, C_FREE1, C_QA, C_VB, C_PB, C_AGG, C_AGG2, C_LATE, C_T0 };
constexpr unsigned SPIN_CAP = 1u << 17;

#define STAMP(i) do { } while (0)

struct Params {
  const u16 *wqA, *xA, *kA, *vA, *woA, *xB, *kB, *vB, *wqB, *woB;
  u16* out;
  unsigned char* ws;
};

template <int V> struct IC { static constexpr int value = V; };
template <int A, int B, typename F> INL void static_for(F&& f) { if constexpr (A < B) { f(IC<A>{}); static_for<A + 1, B>(f); } }
INL float bf2f(u16 v) { return __uint_as_float(((unsigned)v) << 16); }
INL u16 f2bf(float f) { return __builtin_bit_cast(u16, (__bf16)f); }
INL float bf16r(float f) { return bf2f(f2bf(f)); }
INL unsigned pack2(float lo, float hi) { return __builtin_bit_cast(unsigned, __builtin_convertvector(f32x2{lo, hi}, bf16x2)); }
INL float lo16(unsigned u) { return __uint_as_float(u << 16); }
INL float hi16(unsigned u) { return __uint_as_float(u & 0xffff0000u); }
template <int CTRL> INL float dppf(float x) { return __builtin_bit_cast(float, __builtin_amdgcn_mov_dpp(__builtin_bit_cast(int, x), CTRL, 0xf, 0xf, true)); }
template <int CTRL> INL unsigned dppu(unsigned x) { return (unsigned)__builtin_amdgcn_mov_dpp((int)x, CTRL, 0xf, 0xf, true); }
constexpr int XOR1 = 0xB1, XOR2 = 0x4E, ROR4 = 0x124, ROR8 = 0x128, BCAST = 0x150;
INL float x16_max(float x) { auto s = __builtin_amdgcn_permlane16_swap(__float_as_uint(x), __float_as_uint(x), false, false); return fmaxf(__uint_as_float(s[0]), __uint_as_float(s[1])); }
INL float x32_max(float x) { auto s = __builtin_amdgcn_permlane32_swap(__float_as_uint(x), __float_as_uint(x), false, false); return fmaxf(__uint_as_float(s[0]), __uint_as_float(s[1])); }
INL float x16_sum(float x) { auto s = __builtin_amdgcn_permlane16_swap(__float_as_uint(x), __float_as_uint(x), false, false); return __uint_as_float(s[0]) + __uint_as_float(s[1]); }
INL float x32_sum(float x) { auto s = __builtin_amdgcn_permlane32_swap(__float_as_uint(x), __float_as_uint(x), false, false); return __uint_as_float(s[0]) + __uint_as_float(s[1]); }
INL f32x4 mfma16(u32x4 a, u32x4 b, f32x4 c) { return __builtin_amdgcn_mfma_f32_16x16x32_bf16(__builtin_bit_cast(bf16x8, a), __builtin_bit_cast(bf16x8, b), c, 0, 0, 0); }
INL float ex2(float x) { return __builtin_amdgcn_exp2f(x); }
INL float mulL(float m) { float r = m * LOG2E; asm volatile("" : "+v"(r)); return r; }
INL u32x4 asu(const f32x4& v) { return __builtin_bit_cast(u32x4, v); }
INL f32x4 asf(const u32x4& v) { return __builtin_bit_cast(f32x4, v); }
INL float sel4(const f32x4& d, int g) { return (g & 2) ? ((g & 1) ? d[3] : d[2]) : ((g & 1) ? d[1] : d[0]); }
template <typename T> INL void pin(T& d) { asm volatile("" : "+v"(d)); }

INL u32x4 lds128(unsigned a) { return *(const LAS u32x4*)a; }
INL f32x2 lds64f(unsigned a) { return *(const LAS f32x2*)a; }
INL u32x2 lds_tr(unsigned a) { return __builtin_bit_cast(u32x2, __builtin_amdgcn_ds_read_tr16_b64_v4i16((LAS s16x4*)a)); }
INL void lds_signal(int c) {
  u64 keep;
  asm volatile("s_waitcnt lgkmcnt(0)\n\ts_mov_b64 %0, exec\n\ts_mov_b64 exec, 1\n\tds_add_u32 %1, %2\n\ts_mov_b64 exec, %0" : "=&s"(keep) : "v"(L_CNT + 4 * c), "v"(1u) : "memory");
}
INL unsigned lds_ticket(int c) {
  unsigned old; u64 keep;
  asm volatile("s_waitcnt lgkmcnt(0)\n\ts_mov_b64 %1, exec\n\ts_mov_b64 exec, 1\n\tds_add_rtn_u32 %0, %2, %3\n\ts_mov_b64 exec, %1\n\ts_waitcnt lgkmcnt(0)"
               : "=&v"(old), "=&s"(keep) : "v"(L_CNT + 4 * c), "v"(1u) : "memory");
  return __builtin_amdgcn_readfirstlane(old);
}
INL void lds_wait(int c, unsigned n) {
#pragma unroll 1
  for (unsigned s = 0; s < SPIN_CAP * 8; ++s) {
    if ((unsigned)__builtin_amdgcn_readfirstlane(*(volatile LAS unsigned*)(L_CNT + 4 * c)) >= n) break;
    __builtin_amdgcn_s_sleep(1);
  }
  asm volatile("" ::: "memory");
}
INL void wg_barrier() { asm volatile("s_waitcnt lgkmcnt(0)\n\ts_barrier" ::: "memory"); }
INL void nap(int n) {
#pragma unroll 1
  for (int i = 0; i < n; ++i) __builtin_amdgcn_s_sleep(64);
}

template <int OFF> INL void aload(u32x4& d, unsigned voff, const void* sbase) {
  asm volatile("global_load_dwordx4 %0, %1, %2 offset:%3 nt" : "+v"(d) : "v"(voff), "s"(sbase), "i"(OFF) : "memory");
}
template <int N> INL void waitv(u32x4& d) { asm volatile("s_waitcnt vmcnt(%1)" : "+v"(d) : "i"(N) : "memory"); }
template <int N> INL void waitvm() { asm volatile("s_waitcnt vmcnt(%0)" :: "i"(N) : "memory"); }
INL void pacev(u32x4& d, unsigned depth) {
  asm volatile("s_cmp_eq_u32 %1, 7\n\ts_cbranch_scc1 7f\n\ts_cmp_eq_u32 %1, 6\n\ts_cbranch_scc1 6f\n\ts_cmp_eq_u32 %1, 5\n\ts_cbranch_scc1 5f\n\t"
               "s_waitcnt vmcnt(4)\n5:\n\ts_waitcnt vmcnt(5)\n6:\n\ts_waitcnt vmcnt(6)\n7:\n\ts_waitcnt vmcnt(7)" : "+v"(d) : "s"(depth) : "memory", "scc");
}
template <int W, int OFF> INL void stage(u32x4& d, unsigned lds_addr) {
  asm volatile("s_waitcnt vmcnt(%2)\n\tds_write_b128 %1, %0 offset:%3" : "+v"(d) : "v"(lds_addr), "i"(W), "i"(OFF) : "memory");
}

INL float dec(unsigned v) { return __uint_as_float(v); }
INL void put2(const unsigned char* ws, unsigned off, float a, float b, unsigned tag, u64 mask) {
  u64 keep;
  asm volatile("s_mov_b64 %0, exec\n\ts_mov_b64 exec, %4\n\tglobal_store_dwordx4 %1, %2, %3 sc1\n\ts_mov_b64 exec, %0\n\ts_nop 0"
               : "=&s"(keep) : "v"(off), "v"(u32x4{__float_as_uint(a), tag, __float_as_uint(b), tag}), "s"(ws), "s"(mask) : "memory");
}
INL void ld16m(u32x4& d, const unsigned char* ws, unsigned off, u64 mask) {
  asm volatile("s_mov_b64 exec, %3\n\tglobal_load_dwordx4 %0, %1, %2 sc1\n\ts_mov_b64 exec, -1" : "+v"(d) : "v"(off), "s"(ws), "s"(mask) : "memory");
}
template <int N, int SLEEP> INL void gather(u32x4 (&v)[N], const unsigned char* ws, const unsigned (&off)[N], unsigned tag) {
  u64 need[N];
#pragma unroll
  for (int i = 0; i < N; ++i) { asm volatile("" : "=v"(v[i])); need[i] = ~0ull; }
#pragma unroll 1
  for (unsigned s = 0; s < SPIN_CAP; ++s) {
#pragma unroll
    for (int i = 0; i < N; ++i) ld16m(v[i], ws, off[i], need[i]);
    waitvm<0>();
    unsigned bad[N], any = 0;
#pragma unroll
    for (int i = 0; i < N; ++i) pin(v[i]);
#pragma unroll
    for (int i = 0; i < N; ++i) { bad[i] = (v[i][1] ^ tag) | (v[i][3] ^ tag); any |= bad[i]; }
    pin(any);
    if (__builtin_amdgcn_ballot_w64(any != 0) == 0) break;
#pragma unroll
    for (int i = 0; i < N; ++i) {
      const u64 m = __builtin_amdgcn_ballot_w64(bad[i] != 0);
      asm volatile("s_cmp_lg_u64 %1, 0\n\ts_cselect_b64 %0, -1, 0" : "=s"(need[i]) : "s"(m) : "scc");
    }
    if (SLEEP) __builtin_amdgcn_s_sleep(SLEEP);
  }
}
INL void publish256(const unsigned char* ws, unsigned off, int cnt, int lane, unsigned tag) {
  if (lds_ticket(cnt) == 3) {
    const f32x2 e0 = lds64f(L_OA + lane * 8), e1 = lds64f(L_OA + 512 + lane * 8);
    put2(ws, off + lane * 16, e0[0], e0[1], tag, ~0ull);
    put2(ws, off + 1024 + lane * 16, e1[0], e1[1], tag, ~0ull);
  }
}
INL void store_bf16(u16* out, unsigned byte_off, float v) { asm volatile("global_store_short %0, %1, %2 sc1" :: "v"(byte_off), "v"(pack2(v, v)), "s"(out) : "memory"); }

INL unsigned score_step(u32x4 (&R)[8], const u32x4 (&QB)[4], unsigned voff, const char* next, unsigned depth) {
  f32x4 s[2] = {{0.f, 0.f, 0.f, 0.f}, {0.f, 0.f, 0.f, 0.f}};
  static_for<0, 8>([&](auto ic) __attribute__((always_inline)) {
    constexpr int i = decltype(ic)::value;
    pacev(R[i], depth);
    s[i >> 2] = mfma16(R[i], QB[i & 3], s[i >> 2]);
    __builtin_amdgcn_sched_barrier(0);
    aload<1024 * i - 4096>(R[i], voff, next);
    __builtin_amdgcn_sched_barrier(0);
  });
  float d = dppf<0x00>(s[0][0]); d += dppf<0x55>(s[0][1]); d += dppf<0xAA>(s[0][2]); d += dppf<0xFF>(s[0][3]);
  float e = dppf<0x00>(s[1][0]); e += dppf<0x55>(s[1][1]); e += dppf<0xAA>(s[1][2]); e += dppf<0xFF>(s[1][3]);
  const unsigned u = pack2(d, e);
  return pack2(lo16(u) * SCALE, hi16(u) * SCALE);
}
template <bool MORE>
INL void value_step(f32x4& acc, float& l, float mL, unsigned sc, u32x4 (&R)[8], const unsigned (&msk)[4], unsigned voff, const char* next, unsigned depth) {
  const float pa = ex2(__builtin_fmaf(lo16(sc), LOG2E, -mL)), pb = ex2(__builtin_fmaf(hi16(sc), LOG2E, -mL));
  l += pa + pb;
  unsigned pk[2] = {pack2(pa, pa), pack2(pb, pb)};
  pin(pk[0]); pin(pk[1]);
  static_for<0, 8>([&](auto ic) __attribute__((always_inline)) {
    constexpr int i = decltype(ic)::value;
    const unsigned b = pk[i >> 2];
    const u32x4 P = {dppu<BCAST + 4 * (i & 3)>(b) & msk[0], dppu<BCAST + 4 * (i & 3)>(b) & msk[1], dppu<BCAST + 4 * (i & 3)>(b) & msk[2], dppu<BCAST + 4 * (i & 3)>(b) & msk[3]};
    if constexpr (MORE) pacev(R[i], depth); else waitv<7 - i>(R[i]);
    acc = mfma16(R[i], P, acc);
    if constexpr (MORE) { __builtin_amdgcn_sched_barrier(0); aload<1024 * i - 4096>(R[i], voff, next); }
  });
}
INL unsigned pace_depth(unsigned& due, int& prev, unsigned step) {
  due += step;
  int late = (int)((unsigned)__builtin_amdgcn_s_memrealtime() * 16u - due) >> 4;
  if (JUMP != 0 && late - prev > JUMP) { due += (unsigned)(late - prev) * 16u; late = prev; }
  prev = late;
  return __builtin_amdgcn_readfirstlane(min(max(((late >> DSH) + 1) + DLO, DLO), DHI));
}

INL void load_wfrag(u32x4 (&B)[8], unsigned region, int w, int lane) {
  const int g = lane >> 4, q = (lane >> 2) & 3, pp = lane & 3;
  const unsigned sw = 2u * ((q >> 1) | ((g & 1) << 1));
  const unsigned a = region + (8 * g + q) * 128 + 16 * ((2 * w + (pp >> 1)) ^ sw) + 8 * (pp & 1);
#pragma unroll
  for (int kk = 0; kk < 8; ++kk) { const u32x2 lo = lds_tr(a + kk * 4096), hi = lds_tr(a + kk * 4096 + 512); B[kk] = u32x4{lo[0], lo[1], hi[0], hi[1]}; }
}
INL f32x4 piece(unsigned a, const u32x4 (&B)[8], f32x4 d) {
  u32x4 A[8];
#pragma unroll
  for (int kk = 0; kk < 8; ++kk) A[kk] = lds128(a + 64 * kk);
  __builtin_amdgcn_sched_barrier(0);
#pragma unroll
  for (int kk = 0; kk < 8; ++kk) d = mfma16(A[kk], B[kk], d);
  return d;
}
INL f32x4 piece4(int lane, int w) {
  f32x4 d = {0.f, 0.f, 0.f, 0.f};
#pragma unroll 1
  for (int tt = 0; tt < 4; ++tt) { u32x4 B[8]; load_wfrag(B, ((tt + 2) & 3) * 32768, w, lane); d = piece(L_VB + tt * 512 + (lane & 3) * 2112 + 16 * (lane >> 4), B, d); }
  return d;
}
INL unsigned wtile_voff(int lane, int odd, unsigned rs) {
  const unsigned sw = 2u * (((lane >> 4) & 1) | (odd << 1));
  return (lane >> 3) * rs + 16 * ((lane & 7) ^ sw);
}

__global__ void __launch_bounds__(NTHR, 2) fused_decode(Params p) {
  const int lane = threadIdx.x & 63, wave = __builtin_amdgcn_readfirstlane(threadIdx.x >> 6);
  const int j = blockIdx.x, g = lane >> 4, c = lane & 15;
  const int jo = ((j & 15) << 4) | (j >> 4);
  const int ja = j;
  const int jq = j ^ 8, qd = j >> 2, job = (j ^ (j >> 3)) & 3, idx = 2 * qd + (job & 1);

  if (wave >= 4) {
    const int w = wave - 4;
    const unsigned th = (unsigned)__builtin_amdgcn_s_memrealtime();
    u32x4 xv = {0u, 0u, 0u, 0u};
    if (w < 2) asm volatile("global_load_dwordx4 %0, %1, %2" : "+v"(xv) : "v"((lane >> 5) * DM * 2 + (lane & 31) * 16), "s"((const char*)p.xA + (w * 2 * DM + (j & 3) * 256) * 2) : "memory");
    if (w == 3 && lane < 16) *(LAS unsigned*)(L_CNT + 4 * lane) = 0u;
    u64 epoch;
    asm volatile("s_load_dwordx2 %0, %1, 0x0\n\ts_waitcnt lgkmcnt(0)" : "=s"(epoch) : "s"(p.ws + WS_EPOCH) : "memory");
    u64 z = epoch + 0x9E3779B97F4A7C15ull;
    z = (z ^ (z >> 30)) * 0xBF58476D1CE4E5B9ull; z = (z ^ (z >> 27)) * 0x94D049BB133111EBull; z ^= z >> 31;
    const unsigned tag = (unsigned)(z >> 32);
    if (TBASE != 0 && w == 2) put2(p.ws, WS_T0 + jq * 16, dec(th), dec(th), tag, 1ull);
    if (w < 2) { waitv<0>(xv); *(LAS u32x4*)(L_XA + w * 1024 + lane * 16) = xv; }
    wg_barrier();
    if (w == 0) STAMP(3);
    u32x4 F[8];
    load_wfrag(F, L_R0, w, lane);
    *(LAS float*)(L_OA + (g * 64 + 16 * w + c) * 4) = sel4(piece(L_XA + (lane & 3) * 512 + 16 * g, F, f32x4{0.f, 0.f, 0.f, 0.f}), g);
    publish256(p.ws, WS_QA + jq * 2048, C_AGG, lane, tag);
    lds_signal(C_FREE0);
    lds_wait(C_WOA, 4);
    load_wfrag(F, L_R1, w, lane);
#pragma unroll
    for (int kk = 0; kk < 8; ++kk) pin(F[kk]);
    lds_signal(C_FREE1);
    if (w == 0) {
      constexpr int NQ = TBASE != 0 ? 5 : 4;
      u32x4 v[NQ]; unsigned off[NQ];
#pragma unroll
      for (int i = 0; i < 4; ++i) off[i] = WS_QA + (8 * ((ja >> 1) & 31) + 4 * (lane >> 5) + i) * 2048 + (ja >> 6) * 512 + (lane & 31) * 16;
      if (TBASE != 0) off[NQ - 1] = WS_T0 + (8 * ((ja >> 1) & 31) + (lane & 7)) * 16;
      gather<NQ, QA_SLEEP>(v, p.ws, off, tag);
      if (TBASE != 0) {
        int dt = (int)(v[NQ - 1][0] - th);
        if (TBASE == 1) { dt += (int)dppu<XOR1>(dt); dt += (int)dppu<XOR2>(dt); dt += (int)dppu<0x141>(dt); dt >>= 3; }
        else { dt = min(dt, (int)dppu<XOR1>(dt)); dt = min(dt, (int)dppu<XOR2>(dt)); dt = min(dt, (int)dppu<0x141>(dt)); }
        *(LAS unsigned*)(L_CNT + 4 * C_T0) = th + dt;
      }
      *(LAS unsigned*)(L_QA + 4 * lane) = pack2((dec(v[0][0]) + dec(v[1][0])) + (dec(v[2][0]) + dec(v[3][0])), (dec(v[0][2]) + dec(v[1][2])) + (dec(v[2][2]) + dec(v[3][2])));
      lds_signal(C_QA);
      STAMP(10);
    }
    if (job >= 2) {
      {
        nap(NAP_Q);
        u32x4 v[1]; const unsigned off[1] = {WS_QB + (((idx >> 3) & 3) * 16 + 4 * w + g) * 1024 + (idx >> 5) * 256 + c * 16};
        gather<1, 64>(v, p.ws, off, tag);
        *(LAS u32x2*)(L_VB + (2 * w + (lane >> 5)) * 272 + (lane & 31) * 8) = u32x2{v[0][0], v[0][2]};
        lds_signal(C_VB);
      }
      lds_wait(C_VB, 4);
      lds_wait(C_B, 4);
      u32x4 Q[4];
#pragma unroll
      for (int kk = 0; kk < 4; ++kk) Q[kk] = lds128(L_VB + (c & 7) * 272 + 64 * kk + 16 * g);
      float s[16][4]; float mx = -INFINITY;
#pragma unroll
      for (int blk = 0; blk < 16; ++blk) {
        f32x4 d = {0.f, 0.f, 0.f, 0.f};
#pragma unroll
        for (int kk = 0; kk < 4; ++kk) d = mfma16(lds128(L_R2 + (16 * blk + c) * 256 + 16 * ((4 * kk + g) ^ ((c & 3) << 2))), Q[kk], d);
#pragma unroll
        for (int r = 0; r < 4; ++r) { s[blk][r] = bf16r(bf16r(d[r]) * SCALE); mx = fmaxf(mx, s[blk][r]); }
      }
      const float mL = mulL(x32_max(x16_max(mx)));
      float l = 0.f;
#pragma unroll
      for (int blk = 0; blk < 16; ++blk)
#pragma unroll
        for (int r = 0; r < 4; ++r) { s[blk][r] = ex2(__builtin_fmaf(s[blk][r], LOG2E, -mL)); l += s[blk][r]; }
      l = x32_sum(x16_sum(l));
      f32x4 o0 = {0.f, 0.f, 0.f, 0.f}, o1 = {0.f, 0.f, 0.f, 0.f};
      const int q = (lane >> 2) & 3, pp = lane & 3;
      const unsigned va0 = L_R0 + (4 * g + q) * 256 + 16 * ((4 * w + (pp >> 1)) ^ (q << 2)) + 8 * (pp & 1);
#pragma unroll
      for (int tt = 0; tt < 8; ++tt) {
        const u32x4 P = {pack2(s[2 * tt][0], s[2 * tt][1]), pack2(s[2 * tt][2], s[2 * tt][3]), pack2(s[2 * tt + 1][0], s[2 * tt + 1][1]), pack2(s[2 * tt + 1][2], s[2 * tt + 1][3])};
        const u32x2 a0 = lds_tr(va0 + tt * 8192), a1 = lds_tr(va0 + tt * 8192 + 4096), b0 = lds_tr(va0 + tt * 8192 + 32), b1 = lds_tr(va0 + tt * 8192 + 4096 + 32);
        o0 = mfma16(u32x4{a0[0], a0[1], a1[0], a1[1]}, P, o0);
        o1 = mfma16(u32x4{b0[0], b0[1], b1[0], b1[1]}, P, o1);
      }
      asm volatile("global_store_dwordx4 %0, %1, %2 sc1 nt\n\ts_waitcnt vmcnt(0)" :: "v"(WS_PB + idx * PB_REC + w * 1024 + (c & 7) * 128 + (c >> 3) * 64 + g * 16), "v"(asu(c < 8 ? o0 : o1)), "s"(p.ws) : "memory");
      if (lds_ticket(C_PB) == 3) put2(p.ws, WS_PB + idx * PB_REC + 4096 + (lane & 7) * 16, mL, l, tag, 0xffull);
    } else {
      if (job == 0) {
        u32x4 v[2] = {{0u, 0u, 0u, 0u}, {0u, 0u, 0u, 0u}};
        asm volatile("global_load_dwordx4 %0, %2, %3\n\tglobal_load_dwordx4 %1, %2, %3 offset:1024\n\ts_waitcnt vmcnt(0)" : "+v"(v[0]), "+v"(v[1]) : "v"(lane * 16), "s"(p.xB + w * DM) : "memory");
        *(LAS u32x4*)(L_VB + w * 2112 + lane * 16) = v[0]; *(LAS u32x4*)(L_VB + w * 2112 + 1024 + lane * 16) = v[1];
        lds_signal(C_VB);
        lds_wait(C_VB, 4);
        lds_wait(C_B, 4);
        *(LAS u16*)(L_OA + (g * 64 + 16 * w + c) * 2) = f2bf(sel4(piece4(lane, w), g));
        if (lds_ticket(C_AGG2) == 3) { const f32x2 e = lds64f(L_OA + lane * 8); put2(p.ws, WS_QB + qd * 1024 + lane * 16, e[0], e[1], tag, ~0ull); }
      }
      if (w == 3) {
        nap(NAP_M);
        const unsigned rec = WS_PB + ((idx & ~7) + (lane >> 5)) * PB_REC;
        u32x4 ml[1]; const unsigned off[1] = {rec + 2 * (lane & 3) * PB_REC + 4096 + (idx & 7) * 16};
        gather<1, 64>(ml, p.ws, off, tag);
        u32x4 v[4];
        const unsigned po = rec + ((lane >> 3) & 3) * 1024 + (idx & 7) * 128 + (lane & 7) * 16;
        asm volatile("global_load_dwordx4 %0, %4, %8 sc1 nt\n\tglobal_load_dwordx4 %1, %5, %8 sc1 nt\n\tglobal_load_dwordx4 %2, %6, %8 sc1 nt\n\tglobal_load_dwordx4 %3, %7, %8 sc1 nt\n\ts_waitcnt vmcnt(0)"
                     : "=&v"(v[0]), "=&v"(v[1]), "=&v"(v[2]), "=&v"(v[3]) : "v"(po), "v"(po + 2 * PB_REC), "v"(po + 4 * PB_REC), "v"(po + 6 * PB_REC), "s"(p.ws) : "memory");
        const float m = dec(ml[0][0]);
        float M = fmaxf(m, dppf<XOR1>(m)); M = fmaxf(M, dppf<XOR2>(M)); M = x32_max(M);
        const float wt = ex2(m - M);
        float L = wt * dec(ml[0][2]); L += dppf<XOR1>(L); L += dppf<XOR2>(L); L = x32_sum(L);
        f32x4 a = asf(v[0]) * dppf<0x00>(wt) + asf(v[1]) * dppf<0x55>(wt) + asf(v[2]) * dppf<0xAA>(wt) + asf(v[3]) * dppf<0xFF>(wt);
#pragma unroll
        for (int r = 0; r < 4; ++r) a[r] = x32_sum(a[r]) / L;
        put2(p.ws, WS_OB + ((((idx >> 3) & 3) * 4 + (idx >> 5)) * 8 + (idx & 7)) * 512 + (lane & 31) * 16, dec(pack2(a[0], a[1])), dec(pack2(a[2], a[3])), tag, 0xffffffffull);
      }
      if (job == 1) {
        nap(w == 3 ? NAP_O - NAP_M : NAP_O);
        u32x4 v[4]; unsigned off[4];
#pragma unroll
        for (int i = 0; i < 4; ++i) off[i] = WS_OB + ((qd >> 4) * 4 + w) * 4096 + i * 1024 + lane * 16;
        gather<4, 64>(v, p.ws, off, tag);
#pragma unroll
        for (int i = 0; i < 4; ++i) *(LAS u32x2*)(L_VB + w * 2112 + i * 512 + lane * 8) = u32x2{v[i][0], v[i][2]};
        lds_signal(C_VB);
        lds_wait(C_VB, 4);
        lds_wait(C_B, 4);
        const f32x4 d = piece4(lane, w);
        if (g < 2) *(LAS f32x2*)(L_OA + w * 256 + (lane & 31) * 8) = g ? f32x2{d[2], d[3]} : f32x2{d[0], d[1]};
        publish256(p.ws, WS_YB + qd * 2048, C_AGG2, lane, tag);
        if (w == 0) {
          nap(2);
          u32x4 y[2];
          const unsigned base = WS_YB + ((lane >> 5) * 16 + (qd & 15)) * 2048 + (qd >> 4) * 512 + (lane & 31) * 16, yo[2] = {base, base + 32 * 2048};
          gather<2, 64>(y, p.ws, yo, tag);
          const float a = x32_sum(dec(y[0][0]) + dec(y[1][0])), b = x32_sum(dec(y[0][2]) + dec(y[1][2]));
          store_bf16(p.out, ((2 * (g & 1) + (g >> 1)) * 2 * DM + DM + 64 * (qd & 15) + 16 * (qd >> 4) + c) * 2, g < 2 ? a : b);
        }
      }
    }
    if (w == 1) STAMP(5);
    unsigned off[8], yo[2], pa = WS_PA + (8 * ((ja >> 1) & 31) + 2 * (ja >> 6) + (ja & 1)) * PA_REC + (w ? 1024 + (lane & 7) * 16 : lane * 16);
#pragma unroll
    for (int i = 0; i < 4; ++i) { const unsigned rec = WS_PA + ((jo & ~15) + 8 * (i >> 1) + 2 * w + (i & 1)) * PA_REC; off[i] = rec + lane * 16; off[4 + i] = rec + 1024 + (lane & 7) * 16; pin(off[i]); pin(off[4 + i]); }
    const int n = 64 * (jo & 15) + 16 * w + c;
    unsigned ya = WS_YA + ((g & 1) * 128 + (n >> 3)) * 2048 + (jo >> 4) * 128 + (n & 7) * 16;
    unsigned oa = ((2 * (j >> 7) + ((lane >> 3) & 1)) * 2 * DM + 8 * (j & 127) + (lane & 7)) * 2;
    unsigned lp = L_PART + 64 + 8 * lane, lw = L_OA + w * 576 + 4 * lane, lr = L_OA + (lane & 3) * 576 + 16 * g;
    yo[0] = WS_YA + j * 2048 + lane * 16; yo[1] = yo[0] + 1024;
    pin(pa); pin(ya); pin(oa); pin(lp); pin(lw); pin(lr); pin(yo[0]); pin(yo[1]);
    if (TOUCH != 0) {
      if (TOUCH == 2) lds_wait(C_LATE, 1);
      u32x4 dum;
      asm volatile("global_load_dwordx4 %0, %1, %2 sc1\n\ts_waitcnt vmcnt(0)" : "=&v"(dum) : "v"(w < 2 ? pa : yo[w & 1]), "s"(p.ws) : "memory");
    }
    waitvm<0>();
    wg_barrier();
    if (w < 2) {
      if (w == 0) STAMP(6);
      const f32x4 m01 = asf(lds128(L_PART)), m23 = asf(lds128(L_PART + 16));
      f32x2 oo[4];
#pragma unroll
      for (int i = 0; i < 4; ++i) oo[i] = lds64f(lp + i * 512);
      __builtin_amdgcn_sched_barrier(0);
      const float M = fmaxf(fmaxf(m01[0], m01[2]), fmaxf(m23[0], m23[2]));
      const float w0 = ex2(m01[0] - M), w1 = ex2(m01[2] - M), w2 = ex2(m23[0] - M), w3 = ex2(m23[2] - M);
#pragma unroll
      for (int i = 0; i < 4; ++i) pin(oo[i]);
      if (w == 0) put2(p.ws, pa, (w0 * oo[0][0] + w1 * oo[1][0]) + (w2 * oo[2][0] + w3 * oo[3][0]), (w0 * oo[0][1] + w1 * oo[1][1]) + (w2 * oo[2][1] + w3 * oo[3][1]), tag, ~0ull);
      else put2(p.ws, pa, M, (w0 * m01[1] + w1 * m01[3]) + (w2 * m23[1] + w3 * m23[3]), tag, 0xffull);
      if (w == 0) STAMP(19);
    }
    u32x4 v[8];
    gather<8, PA_SLEEP>(v, p.ws, off, tag);
    STAMP(20 + w);
#pragma unroll
    for (int hh = 0; hh < 2; ++hh) {
      const float m0 = dec(v[4 + 2 * hh][0]), m1 = dec(v[5 + 2 * hh][0]), M = fmaxf(m0, m1);
      const float w0 = ex2(m0 - M), w1 = ex2(m1 - M);
      const float rl = __builtin_amdgcn_rcpf(w0 * dec(v[4 + 2 * hh][2]) + w1 * dec(v[5 + 2 * hh][2]));
      const float a0 = w0 * dec(v[2 * hh][0]) + w1 * dec(v[2 * hh + 1][0]), a1 = w0 * dec(v[2 * hh][2]) + w1 * dec(v[2 * hh + 1][2]);
      *(LAS unsigned*)(lw + 256 * hh) = pack2(a0 * rl, a1 * rl);
    }
    wg_barrier();
    const f32x4 d = piece(lr, F, f32x4{0.f, 0.f, 0.f, 0.f});
    put2(p.ws, ya, g ? d[2] : d[0], g ? d[3] : d[1], tag, 0xffffffffull);
    if (w == 0) {
      STAMP(8);
      u32x4 y[2];
      gather<2, Y_SLEEP>(y, p.ws, yo, tag);
      const float a = dec(y[0][0]) + dec(y[1][0]), b = dec(y[0][2]) + dec(y[1][2]);
      const float k = (lane & 8) ? b : a, sd = (lane & 8) ? a : b;
      const float r = x32_sum(x16_sum(k + dppf<ROR8>(sd)));
      if (lane < 16) store_bf16(p.out, oa, r);
      STAMP(9);
      if (j == 0 && lane == 0) __hip_atomic_store((GAS u64*)(p.ws + WS_EPOCH), epoch + 1, __ATOMIC_RELAXED, __HIP_MEMORY_SCOPE_AGENT);
      { const unsigned tb = PROBE == 1 ? th : (unsigned)__builtin_amdgcn_readfirstlane(*(const volatile LAS unsigned*)(L_CNT + 4 * C_T0));
        const unsigned lim = 11000u + (PROBE == 3 ? 4u * ((unsigned)__builtin_amdgcn_s_memrealtime() - tb - 7800u) : 0u);
        for (unsigned it = 0; it < 100000; ++it) { if ((unsigned)__builtin_amdgcn_s_memrealtime() - tb >= lim) break; __builtin_amdgcn_s_sleep(1); } }
    }
    return;
  }

  if (wave == 0) STAMP(0);
  const unsigned vq0 = wtile_voff(lane, 0, HD * 2), vq1 = wtile_voff(lane, 1, HD * 2);
  const size_t qoff = ((size_t)((j & 3) * 256 + wave * 64) * HD + (jq >> 2) * 64) * 2;
  u32x4 R[8];
  static_for<0, 8>([&](auto ic) __attribute__((always_inline)) {
    constexpr int i = decltype(ic)::value;
    u32x4& r = R[i];
    asm volatile("" : "=v"(r));
    aload<0>(r, (i & 1) ? vq1 : vq0, (const char*)p.wqA + qoff + (size_t)i * 8 * HD * 2);
  });
  unsigned t0 = (unsigned)__builtin_amdgcn_s_memrealtime();
  int jb = job; asm volatile("" : "+s"(jb));
  const unsigned vo0 = wtile_voff(lane, 0, DM * 2), vo1 = wtile_voff(lane, 1, DM * 2), vkv = g * 256 + 16 * (c ^ (g << 2));
  const size_t ooff = ((size_t)((jo >> 4) * 256 + wave * 64) * DM + (jo & 15) * 64) * 2;
  const unsigned vb0 = jb == 0 ? vq0 : jb == 1 ? vo0 : vkv, vb1 = jb == 0 ? vq1 : jb == 1 ? vo1 : vkv;
  unsigned bstep = jb == 0 ? 8 * HD * 2 : jb == 1 ? 8 * DM * 2 : 1024;
  asm volatile("" : "+s"(bstep));
  const unsigned boff = (jb == 0 ? qd * 128 : jb == 1 ? ((qd >> 4) * 1024 * DM + (qd & 15) * 64) * 2
                                                      : ((idx >> 3) * SK + (SK - WIN) + (idx & 7) * 256) * DH * 2) + wave * 8 * bstep;
  const char* bp = (const char*)(jb == 0 ? p.wqB : jb == 1 ? p.woB : p.kB) + boff;
  const char* const b23 = jb < 2 ? bp + 64 * bstep : (const char*)p.vB + boff;
  const unsigned voffV = lane * 16, voffK = KLIN ? lane * 16 : ((lane >> 2) & 3) * 256 + g * 64 + (lane & 3) * 16;
  const unsigned ldst = wave * 8192 + lane * 16;
  const char* const kp = (const char*)p.kA + (size_t)ja * 1048576 + wave * 8192 + 4096;
  const char* const vp = (const char*)p.vA + (size_t)ja * 1048576 + wave * 8192 + 4096;
  static_for<8, 56>([&](auto nc) __attribute__((always_inline)) {
    constexpr int n = decltype(nc)::value, k = n >> 3, i = n & 7;
    constexpr unsigned reg = ((k - 1) & 3) * 32768;
    if constexpr (n == 40) lds_wait(C_FREE0, 4);
    if constexpr (n == 48) lds_wait(C_FREE1, 4);
    stage<n < 16 ? 7 : PW, (reg & 0xffffu) + i * 1024>(R[i], ldst + (reg & ~0xffffu));
    if constexpr (k == 1) aload<0>(R[i], (i & 1) ? vo1 : vo0, (const char*)p.woA + ooff + (size_t)i * 8 * DM * 2);
    if constexpr (k >= 2 && k <= 5) {
      if constexpr (n == 32) bp = b23;
      aload<0>(R[i], (i & 1) ? vb1 : vb0, bp);
      bp += (i == 7 ? 25 : 1) * bstep;
    }
    if constexpr (k == 6) aload<1024 * i - 4096>(R[i], voffK, kp);
    if constexpr (i == 7 && k < 6) { if (wave == 0) STAMP(23 + k); }
    if constexpr (n == 15) wg_barrier();
    if constexpr (n == 23) lds_signal(C_WOA);
    if constexpr (n == 55) lds_signal(C_B);
  });
  if (wave == 0) STAMP(1);
  lds_wait(C_QA, 1);
  if (wave == 0) STAMP(2);
  if (TBASE != 0) t0 = __builtin_amdgcn_readfirstlane(*(const LAS unsigned*)(L_CNT + 4 * C_T0));
  unsigned due = (t0 + SCH0) * 16u; int prev = 1 << 20;
  const unsigned sca = L_SC + wave * 2048 + (lane >> 2) * 4;
  float mx = -INFINITY;
  {
    u32x4 QB[4];
    const u32x4 qv = lds128(L_QA + 16 * (4 * g + (lane & 3)));
#pragma unroll
    for (int i = 0; i < 4; ++i) QB[i] = ((c >> 2) == i) ? qv : u32x4{0u, 0u, 0u, 0u};
#pragma unroll 1
    for (unsigned r = 1; r <= 32; ++r) {
      if (wave == 0 && (r & 7) == 0) STAMP(10 + (r >> 3));
      const unsigned sc = score_step(R, QB, r == 32 ? voffV : voffK, r == 32 ? vp : kp + r * 32768, pace_depth(due, prev, SCH));
      *(LAS unsigned*)(sca + (r - 1) * 64) = sc;
      mx = fmaxf(mx, fmaxf(lo16(sc), hi16(sc)));
    }
  }
  mx = fmaxf(mx, dppf<ROR4>(mx)); mx = fmaxf(mx, dppf<ROR8>(mx));
  const float mL = mulL(x32_max(x16_max(mx)));
  if (wave == 0) STAMP(4);
  unsigned msk[4];
#pragma unroll
  for (int i = 0; i < 4; ++i) msk[i] = (c == 2 * i) ? 0x0000ffffu : (c == 2 * i + 1) ? 0xffff0000u : 0u;
  f32x4 acc = {0.f, 0.f, 0.f, 0.f}; float l = 0.f;
#pragma unroll 1
  for (unsigned r = 1; r < 32; ++r) {
    if (TOUCH == 2 && r == LATER && wave == 0) *(volatile LAS unsigned*)(L_CNT + 4 * C_LATE) = 1u;
    if (wave == 0 && (r & 7) == 0) STAMP(14 + (r >> 3));
    value_step<true>(acc, l, mL, *(const LAS unsigned*)(sca + (r - 1) * 64), R, msk, voffV, vp + r * 32768, pace_depth(due, prev, SCHV));
  }
  value_step<false>(acc, l, mL, *(const LAS unsigned*)(sca + 31 * 64), R, msk, 0u, nullptr, 0u);
  l += dppf<ROR4>(l); l += dppf<ROR8>(l); l = x32_sum(x16_sum(l));
  if (c < 8) {
#pragma unroll
    for (int r = 0; r < 4; ++r) *(LAS float*)(L_PART + 64 + wave * 512 + (8 * (4 * g + r) + c) * 4) = acc[r];
  }
  if (lane == 0) *(LAS f32x2*)(L_PART + wave * 8) = f32x2{mL, l};
  wg_barrier();
}

extern "C" void kernel_launch(void* const* d_in, const int* in_sizes, int n_in, void* d_out, int out_size, void* d_ws, size_t ws_size, hipStream_t stream) {
  Params p;
  p.xA = (const u16*)d_in[0]; p.kA = (const u16*)d_in[1]; p.vA = (const u16*)d_in[2]; p.wqA = (const u16*)d_in[3]; p.woA = (const u16*)d_in[4];
  p.xB = (const u16*)d_in[5]; p.kB = (const u16*)d_in[6]; p.vB = (const u16*)d_in[7]; p.wqB = (const u16*)d_in[8]; p.woB = (const u16*)d_in[9];
  p.out = (u16*)d_out; p.ws = (unsigned char*)d_ws;
  (void)hipFuncSetAttribute((const void*)fused_decode, hipFuncAttributeMaxDynamicSharedMemorySize, LDS_BYTES);
  fused_decode<<<NWG, NTHR, LDS_BYTES, stream>>>(p);
}
```

```cpp
#include <hip/hip_runtime.h>
#include <hip/hip_bf16.h>
#include <cstdint>
#pragma clang diagnostic ignored "-Wint-to-pointer-cast"

typedef __attribute__((ext_vector_type(4))) unsigned u32x4;
typedef __attribute__((ext_vector_type(2))) unsigned u32x2;
typedef __attribute__((ext_vector_type(4))) float f32x4;
typedef __attribute__((ext_vector_type(2))) float f32x2;
typedef __attribute__((ext_vector_type(8))) __bf16 bf16x8;
typedef __attribute__((ext_vector_type(2))) __bf16 bf16x2;
typedef __attribute__((ext_vector_type(4))) short s16x4;
typedef unsigned short u16;
typedef unsigned long long u64;
#define LAS __attribute__((address_space(3)))
#define GAS __attribute__((address_space(1)))
#define INL __device__ __forceinline__

constexpr int DM = 1024, NH = 32, DH = 128, SK = 8192, WIN = 2048, HD = NH * DH;
constexpr float SCALE = 0.088388347648318440f, LOG2E = 1.4426950408889634f;
constexpr int NWG = 256, NTHR = 512;

#define SCH 1775
#define SCH0 770
#define DLO 4
#define DHI 7
#define DSH 6
#define MAPO 1
#define MAPA 0
#define MAPB 1
#define PA_SLEEP 1
#define Y_SLEEP 0
#define TOUCH 1
#define LATER 16
#define TBASE 1
#define SCHV 1725
#define JUMP 150
#define KLIN 0
#define QSN false
#define QLN false
#define BSN true
#define BLN false
#define PBH 0
#define PRIO 0
#define VT 0
#define VTE 0
#define VTW 2
#define PA_NAP 16
#define Y_NAP 12
#define QA_NAP 0
#define PW0 7
#define PW 5
#define ENDR 0
#define QA_SLEEP 8
#define NAP_Q 6
#define NAP_M 12
#define NAP_O 18

constexpr unsigned WS_EPOCH = 0;
constexpr unsigned WS_T0 = 4096;
constexpr unsigned WS_QA = WS_T0 + NWG * 16;
constexpr unsigned PA_REC = 1024;
constexpr unsigned WS_PA = WS_QA + NWG * 2048;
constexpr unsigned WS_ML = WS_PA + NWG * PA_REC;
constexpr unsigned WS_YA = WS_ML + NWG * 128;
constexpr unsigned WS_QB = WS_YA + NWG * 2048;
constexpr unsigned PB_REC = 4096 + 128;
constexpr unsigned WS_PB = WS_QB + 64 * 1024;
constexpr unsigned WS_OB = WS_PB + 128 * PB_REC;
constexpr unsigned WS_YB = WS_OB + 128 * 512;
constexpr unsigned WS_END = WS_YB + 64 * 2048;
static_assert(WS_END <= (4u << 20), "workspace");

constexpr unsigned L_R0 = 0, L_R1 = 32768, L_R2 = 65536, L_R3 = 98304;
constexpr unsigned L_QA = 131072;
constexpr unsigned L_OA = L_QA + 256;
constexpr unsigned L_PART = L_OA + 4 * 576;
constexpr unsigned L_XA = L_PART + 64 + 4 * 512;
constexpr unsigned L_VB = L_XA + 2048;
constexpr unsigned L_SC = L_VB + 4 * 2112;
constexpr unsigned L_CNT = L_SC + 8192;
constexpr unsigned LDS_BYTES = L_CNT + 64 + 256;
enum { C_WOA = 0, C_B, C_FREE0, C_FREE1, C_QA, C_VB, C_PB, C_AGG, C_AGG2, C_LATE, C_T0, C_KEND, C_ML };
constexpr unsigned SPIN_CAP = 1u << 17;

#define STAMP(i) do { } while (0)

struct Params {
  const u16 *wqA, *xA, *kA, *vA, *woA, *xB, *kB, *vB, *wqB, *woB;
  u16* out;
  unsigned char* ws;
};

template <int V> struct IC { static constexpr int value = V; };
template <int A, int B, typename F> INL void static_for(F&& f) { if constexpr (A < B) { f(IC<A>{}); static_for<A + 1, B>(f); } }
INL float bf2f(u16 v) { return __uint_as_float(((unsigned)v) << 16); }
INL u16 f2bf(float f) { return __builtin_bit_cast(u16, (__bf16)f); }
INL float bf16r(float f) { return bf2f(f2bf(f)); }
INL unsigned pack2(float lo, float hi) { return __builtin_bit_cast(unsigned, __builtin_convertvector(f32x2{lo, hi}, bf16x2)); }
INL float lo16(unsigned u) { return __uint_as_float(u << 16); }
INL float hi16(unsigned u) { return __uint_as_float(u & 0xffff0000u); }
template <int CTRL> INL float dppf(float x) { return __builtin_bit_cast(float, __builtin_amdgcn_mov_dpp(__builtin_bit_cast(int, x), CTRL, 0xf, 0xf, true)); }
template <int CTRL> INL unsigned dppu(unsigned x) { return (unsigned)__builtin_amdgcn_mov_dpp((int)x, CTRL, 0xf, 0xf, true); }
constexpr int XOR1 = 0xB1, XOR2 = 0x4E, ROR4 = 0x124, ROR8 = 0x128, BCAST = 0x150;
INL float x16_max(float x) { auto s = __builtin_amdgcn_permlane16_swap(__float_as_uint(x), __float_as_uint(x), false, false); return fmaxf(__uint_as_float(s[0]), __uint_as_float(s[1])); }
INL float x32_max(float x) { auto s = __builtin_amdgcn_permlane32_swap(__float_as_uint(x), __float_as_uint(x), false, false); return fmaxf(__uint_as_float(s[0]), __uint_as_float(s[1])); }
INL float x16_sum(float x) { auto s = __builtin_amdgcn_permlane16_swap(__float_as_uint(x), __float_as_uint(x), false, false); return __uint_as_float(s[0]) + __uint_as_float(s[1]); }
INL float x32_sum(float x) { auto s = __builtin_amdgcn_permlane32_swap(__float_as_uint(x), __float_as_uint(x), false, false); return __uint_as_float(s[0]) + __uint_as_float(s[1]); }
INL f32x4 mfma16(u32x4 a, u32x4 b, f32x4 c) { return __builtin_amdgcn_mfma_f32_16x16x32_bf16(__builtin_bit_cast(bf16x8, a), __builtin_bit_cast(bf16x8, b), c, 0, 0, 0); }
INL float ex2(float x) { return __builtin_amdgcn_exp2f(x); }
INL float mulL(float m) { float r = m * LOG2E; asm volatile("" : "+v"(r)); return r; }
INL u32x4 asu(const f32x4& v) { return __builtin_bit_cast(u32x4, v); }
INL f32x4 asf(const u32x4& v) { return __builtin_bit_cast(f32x4, v); }
INL float sel4(const f32x4& d, int g) { return (g & 2) ? ((g & 1) ? d[3] : d[2]) : ((g & 1) ? d[1] : d[0]); }
template <typename T> INL void pin(T& d) { asm volatile("" : "+v"(d)); }

INL u32x4 lds128(unsigned a) { return *(const LAS u32x4*)a; }
INL f32x2 lds64f(unsigned a) { return *(const LAS f32x2*)a; }
INL u32x2 lds_tr(unsigned a) { return __builtin_bit_cast(u32x2, __builtin_amdgcn_ds_read_tr16_b64_v4i16((LAS s16x4*)a)); }
INL void lds_signal(int c) {
  u64 keep;
  asm volatile("s_waitcnt lgkmcnt(0)\n\ts_mov_b64 %0, exec\n\ts_mov_b64 exec, 1\n\tds_add_u32 %1, %2\n\ts_mov_b64 exec, %0" : "=&s"(keep) : "v"(L_CNT + 4 * c), "v"(1u) : "memory");
}
INL unsigned lds_ticket(int c) {
  unsigned old; u64 keep;
  asm volatile("s_waitcnt lgkmcnt(0)\n\ts_mov_b64 %1, exec\n\ts_mov_b64 exec, 1\n\tds_add_rtn_u32 %0, %2, %3\n\ts_mov_b64 exec, %1\n\ts_waitcnt lgkmcnt(0)"
               : "=&v"(old), "=&s"(keep) : "v"(L_CNT + 4 * c), "v"(1u) : "memory");
  return __builtin_amdgcn_readfirstlane(old);
}
INL void lds_wait(int c, unsigned n) {
#pragma unroll 1
  for (unsigned s = 0; s < SPIN_CAP * 8; ++s) {
    if ((unsigned)__builtin_amdgcn_readfirstlane(*(volatile LAS unsigned*)(L_CNT + 4 * c)) >= n) break;
    __builtin_amdgcn_s_sleep(1);
  }
  asm volatile("" ::: "memory");
}
INL void wg_barrier() { asm volatile("s_waitcnt lgkmcnt(0)\n\ts_barrier" ::: "memory"); }
INL void nap(int n) {
#pragma unroll 1
  for (int i = 0; i < n; ++i) __builtin_amdgcn_s_sleep(64);
}

template <int OFF> INL void aload(u32x4& d, unsigned voff, const void* sbase) {
  asm volatile("global_load_dwordx4 %0, %1, %2 offset:%3 nt" : "+v"(d) : "v"(voff), "s"(sbase), "i"(OFF) : "memory");
}
template <int N> INL void waitv(u32x4& d) { asm volatile("s_waitcnt vmcnt(%1)" : "+v"(d) : "i"(N) : "memory"); }
template <int N> INL void waitvm() { asm volatile("s_waitcnt vmcnt(%0)" :: "i"(N) : "memory"); }
INL void pacev(u32x4& d, unsigned depth) {
  asm volatile("s_cmp_eq_u32 %1, 7\n\ts_cbranch_scc1 7f\n\ts_cmp_eq_u32 %1, 6\n\ts_cbranch_scc1 6f\n\ts_cmp_eq_u32 %1, 5\n\ts_cbranch_scc1 5f\n\t"
               "s_waitcnt vmcnt(4)\n5:\n\ts_waitcnt vmcnt(5)\n6:\n\ts_waitcnt vmcnt(6)\n7:\n\ts_waitcnt vmcnt(7)" : "+v"(d) : "s"(depth) : "memory", "scc");
}
template <int W, int OFF> INL void stage(u32x4& d, unsigned lds_addr) {
  asm volatile("s_waitcnt vmcnt(%2)\n\tds_write_b128 %1, %0 offset:%3" : "+v"(d) : "v"(lds_addr), "i"(W), "i"(OFF) : "memory");
}

INL float dec(unsigned v) { return __uint_as_float(v); }
template <bool NT = false> INL void put2(const unsigned char* ws, unsigned off, float a, float b, unsigned tag, u64 mask) {
  u64 keep;
  if (NT) asm volatile("s_mov_b64 %0, exec\n\ts_mov_b64 exec, %4\n\tglobal_store_dwordx4 %1, %2, %3 sc1 nt\n\ts_mov_b64 exec, %0\n\ts_nop 0"
               : "=&s"(keep) : "v"(off), "v"(u32x4{__float_as_uint(a), tag, __float_as_uint(b), tag}), "s"(ws), "s"(mask) : "memory");
  else asm volatile("s_mov_b64 %0, exec\n\ts_mov_b64 exec, %4\n\tglobal_store_dwordx4 %1, %2, %3 sc1\n\ts_mov_b64 exec, %0\n\ts_nop 0"
               : "=&s"(keep) : "v"(off), "v"(u32x4{__float_as_uint(a), tag, __float_as_uint(b), tag}), "s"(ws), "s"(mask) : "memory");
}
template <bool NT> INL void ld16m(u32x4& d, const unsigned char* ws, unsigned off, u64 mask) {
  if (NT) asm volatile("s_mov_b64 exec, %3\n\tglobal_load_dwordx4 %0, %1, %2 sc1 nt\n\ts_mov_b64 exec, -1" : "+v"(d) : "v"(off), "s"(ws), "s"(mask) : "memory");
  else asm volatile("s_mov_b64 exec, %3\n\tglobal_load_dwordx4 %0, %1, %2 sc1\n\ts_mov_b64 exec, -1" : "+v"(d) : "v"(off), "s"(ws), "s"(mask) : "memory");
}
template <int N, int SLEEP, bool NT = false> INL void gather(u32x4 (&v)[N], const unsigned char* ws, const unsigned (&off)[N], unsigned tag) {
  u64 need[N];
#pragma unroll
  for (int i = 0; i < N; ++i) { asm volatile("" : "=v"(v[i])); need[i] = ~0ull; }
#pragma unroll 1
  for (unsigned s = 0; s < SPIN_CAP; ++s) {
#pragma unroll
    for (int i = 0; i < N; ++i) ld16m<NT>(v[i], ws, off[i], need[i]);
    waitvm<0>();
    unsigned bad[N], any = 0;
#pragma unroll
    for (int i = 0; i < N; ++i) pin(v[i]);
#pragma unroll
    for (int i = 0; i < N; ++i) { bad[i] = (v[i][1] ^ tag) | (v[i][3] ^ tag); any |= bad[i]; }
    pin(any);
    if (__builtin_amdgcn_ballot_w64(any != 0) == 0) break;
#pragma unroll
    for (int i = 0; i < N; ++i) {
      const u64 m = __builtin_amdgcn_ballot_w64(bad[i] != 0);
      asm volatile("s_cmp_lg_u64 %1, 0\n\ts_cselect_b64 %0, -1, 0" : "=s"(need[i]) : "s"(m) : "scc");
    }
    if (SLEEP) __builtin_amdgcn_s_sleep(SLEEP);
  }
}
template <bool NT> INL void publish256(const unsigned char* ws, unsigned off, int cnt, int lane, unsigned tag) {
  if (lds_ticket(cnt) == 3) {
    const f32x2 e0 = lds64f(L_OA + lane * 8), e1 = lds64f(L_OA + 512 + lane * 8);
    put2<NT>(ws, off + lane * 16, e0[0], e0[1], tag, ~0ull);
    put2<NT>(ws, off + 1024 + lane * 16, e1[0], e1[1], tag, ~0ull);
  }
}
INL void store_bf16(u16* out, unsigned byte_off, float v) { asm volatile("global_store_short %0, %1, %2 sc1" :: "v"(byte_off), "v"(pack2(v, v)), "s"(out) : "memory"); }

INL unsigned score_step(u32x4 (&R)[8], const u32x4 (&QB)[4], unsigned voff, const char* next, unsigned depth) {
  f32x4 s[2] = {{0.f, 0.f, 0.f, 0.f}, {0.f, 0.f, 0.f, 0.f}};
  static_for<0, 8>([&](auto ic) __attribute__((always_inline)) {
    constexpr int i = decltype(ic)::value;
    pacev(R[i], depth);
    s[i >> 2] = mfma16(R[i], QB[i & 3], s[i >> 2]);
    __builtin_amdgcn_sched_barrier(0);
    aload<1024 * i - 4096>(R[i], voff, next);
    __builtin_amdgcn_sched_barrier(0);
  });
  float d = dppf<0x00>(s[0][0]); d += dppf<0x55>(s[0][1]); d += dppf<0xAA>(s[0][2]); d += dppf<0xFF>(s[0][3]);
  float e = dppf<0x00>(s[1][0]); e += dppf<0x55>(s[1][1]); e += dppf<0xAA>(s[1][2]); e += dppf<0xFF>(s[1][3]);
  const unsigned u = pack2(d, e);
  return pack2(lo16(u) * SCALE, hi16(u) * SCALE);
}
template <bool MORE>
INL void value_step(f32x4& acc, float mL, unsigned sc, u32x4 (&R)[8], const unsigned (&msk)[4], unsigned voff, const char* next, unsigned depth) {
  const float pa = ex2(__builtin_fmaf(lo16(sc), LOG2E, -mL)), pb = ex2(__builtin_fmaf(hi16(sc), LOG2E, -mL));
  unsigned pk[2] = {pack2(pa, pa), pack2(pb, pb)};
  pin(pk[0]); pin(pk[1]);
  static_for<0, 8>([&](auto ic) __attribute__((always_inline)) {
    constexpr int i = decltype(ic)::value;
    const unsigned b = pk[i >> 2];
    u32x4 P = {dppu<BCAST + 4 * (i & 3)>(b) & msk[0], dppu<BCAST + 4 * (i & 3)>(b) & msk[1], dppu<BCAST + 4 * (i & 3)>(b) & msk[2], dppu<BCAST + 4 * (i & 3)>(b) & msk[3]};
    if constexpr (MORE) pacev(R[i], depth); else { pin(P); waitv<7 - i>(R[i]); }
    acc = mfma16(R[i], P, acc);
    __builtin_amdgcn_sched_barrier(0);
    if constexpr (MORE) aload<1024 * i - 4096>(R[i], voff, next);
  });
}
INL unsigned pace_depth(unsigned& due, int& prev, unsigned step) {
  due += step;
  int late = (int)((unsigned)__builtin_amdgcn_s_memrealtime() * 16u - due) >> 4;
  if (JUMP != 0 && late - prev > JUMP) { due += (unsigned)(late - prev) * 16u; late = prev; }
  prev = late;
  return __builtin_amdgcn_readfirstlane(min(max(((late >> DSH) + 1) + DLO, DLO), DHI));
}

INL void load_wfrag(u32x4 (&B)[8], unsigned region, int w, int lane) {
  const int g = lane >> 4, q = (lane >> 2) & 3, pp = lane & 3;
  const unsigned sw = 2u * ((q >> 1) | ((g & 1) << 1));
  const unsigned a = region + (8 * g + q) * 128 + 16 * ((2 * w + (pp >> 1)) ^ sw) + 8 * (pp & 1);
#pragma unroll
  for (int kk = 0; kk < 8; ++kk) { const u32x2 lo = lds_tr(a + kk * 4096), hi = lds_tr(a + kk * 4096 + 512); B[kk] = u32x4{lo[0], lo[1], hi[0], hi[1]}; }
}
INL f32x4 piece(unsigned a, const u32x4 (&B)[8], f32x4 d) {
  u32x4 A[8];
#pragma unroll
  for (int kk = 0; kk < 8; ++kk) A[kk] = lds128(a + 64 * kk);
  __builtin_amdgcn_sched_barrier(0);
#pragma unroll
  for (int kk = 0; kk < 8; ++kk) d = mfma16(A[kk], B[kk], d);
  return d;
}
INL f32x4 piece4(int lane, int w) {
  f32x4 d = {0.f, 0.f, 0.f, 0.f};
#pragma unroll 1
  for (int tt = 0; tt < 4; ++tt) { u32x4 B[8]; load_wfrag(B, ((tt + 2) & 3) * 32768, w, lane); d = piece(L_VB + tt * 512 + (lane & 3) * 2112 + 16 * (lane >> 4), B, d); }
  return d;
}
INL unsigned wtile_voff(int lane, int odd, unsigned rs) {
  const unsigned sw = 2u * (((lane >> 4) & 1) | (odd << 1));
  return (lane >> 3) * rs + 16 * ((lane & 7) ^ sw);
}

__global__ void __launch_bounds__(NTHR, 2) fused_decode(Params p) {
  const int lane = threadIdx.x & 63, wave = __builtin_amdgcn_readfirstlane(threadIdx.x >> 6);
  const int j = blockIdx.x, g = lane >> 4, c = lane & 15;
  const int jo = ((j & 15) << 4) | (j >> 4);
  const int ja = j;
  const int jq = j ^ 8, job = (j >> 3) & 3, qd = ((j & 7) << 3) | (j >> 5), idx = job >= 2 ? ((j & 7) << 4) | ((job & 1) << 3) | (j >> 5) : 2 * qd + job;

  if (wave >= 4) {
    const int w = wave - 4;
    const unsigned th = (unsigned)__builtin_amdgcn_s_memrealtime();
    u32x4 xv = {0u, 0u, 0u, 0u};
    if (w < 2) asm volatile("global_load_dwordx4 %0, %1, %2" : "+v"(xv) : "v"((lane >> 5) * DM * 2 + (lane & 31) * 16), "s"((const char*)p.xA + (w * 2 * DM + (j & 3) * 256) * 2) : "memory");
    if (w == 3 && lane < 16) *(LAS unsigned*)(L_CNT + 4 * lane) = 0u;
    u64 epoch;
    asm volatile("s_load_dwordx2 %0, %1, 0x0\n\ts_waitcnt lgkmcnt(0)" : "=s"(epoch) : "s"(p.ws + WS_EPOCH) : "memory");
    u64 z = epoch + 0x9E3779B97F4A7C15ull;
    z = (z ^ (z >> 30)) * 0xBF58476D1CE4E5B9ull; z = (z ^ (z >> 27)) * 0x94D049BB133111EBull; z ^= z >> 31;
    const unsigned tag = (unsigned)(z >> 32);
    if (TBASE != 0 && w == 2) put2(p.ws, WS_T0 + jq * 16, dec(th), dec(th), tag, 1ull);
    if (w < 2) { waitv<0>(xv); *(LAS u32x4*)(L_XA + w * 1024 + lane * 16) = xv; }
    wg_barrier();
    if (w == 0) STAMP(3);
    u32x4 F[8];
    load_wfrag(F, L_R0, w, lane);
    *(LAS float*)(L_OA + (g * 64 + 16 * w + c) * 4) = sel4(piece(L_XA + (lane & 3) * 512 + 16 * g, F, f32x4{0.f, 0.f, 0.f, 0.f}), g);
    publish256<QSN>(p.ws, WS_QA + jq * 2048, C_AGG, lane, tag);
    lds_signal(C_FREE0);
    lds_wait(C_WOA, 4);
    load_wfrag(F, L_R1, w, lane);
#pragma unroll
    for (int kk = 0; kk < 8; ++kk) pin(F[kk]);
    lds_signal(C_FREE1);
    if (w == 0) {
      constexpr int NQ = TBASE != 0 ? 5 : 4;
      u32x4 v[NQ]; unsigned off[NQ];
#pragma unroll
      for (int i = 0; i < 4; ++i) off[i] = WS_QA + (8 * ((ja >> 1) & 31) + 4 * (lane >> 5) + i) * 2048 + (ja >> 6) * 512 + (lane & 31) * 16;
      if (TBASE != 0) off[NQ - 1] = WS_T0 + (8 * ((ja >> 1) & 31) + (lane & 7)) * 16;
      if (QA_NAP != 0) __builtin_amdgcn_s_sleep(QA_NAP);
      gather<NQ, QA_SLEEP, QLN>(v, p.ws, off, tag);
      if (TBASE != 0) {
        int dt = (int)(v[NQ - 1][0] - th);
        if (TBASE == 1) { dt += (int)dppu<XOR1>(dt); dt += (int)dppu<XOR2>(dt); dt += (int)dppu<0x141>(dt); dt >>= 3; }
        else { dt = min(dt, (int)dppu<XOR1>(dt)); dt = min(dt, (int)dppu<XOR2>(dt)); dt = min(dt, (int)dppu<0x141>(dt)); }
        *(LAS unsigned*)(L_CNT + 4 * C_T0) = th + dt;
      }
      *(LAS unsigned*)(L_QA + 4 * lane) = pack2((dec(v[0][0]) + dec(v[1][0])) + (dec(v[2][0]) + dec(v[3][0])), (dec(v[0][2]) + dec(v[1][2])) + (dec(v[2][2]) + dec(v[3][2])));
      lds_signal(C_QA);
      STAMP(10);
    }
    if (VTE != 0 && w == 1) {
      unsigned dum;
      asm volatile("global_load_dword %0, %1, %2 nt\n\ts_waitcnt vmcnt(0)" : "=&v"(dum) : "v"((lane & (VTE - 1)) * (1048576 / (VTE ? VTE : 1))), "s"((const char*)p.vA + (size_t)ja * 1048576) : "memory");
      asm volatile("global_load_dword %0, %1, %2 nt\n\ts_waitcnt vmcnt(0)" : "=&v"(dum) : "v"((lane & (VTE - 1)) * (1048576 / (VTE ? VTE : 1))), "s"((const char*)p.kA + (size_t)ja * 1048576) : "memory");
    }
    if (job >= 2) {
      {
        nap(NAP_Q);
        u32x4 v[1]; const unsigned off[1] = {WS_QB + (((idx >> 3) & 3) * 16 + 4 * w + g) * 1024 + (idx >> 5) * 256 + c * 16};
        gather<1, 64, BLN>(v, p.ws, off, tag);
        *(LAS u32x2*)(L_VB + (2 * w + (lane >> 5)) * 272 + (lane & 31) * 8) = u32x2{v[0][0], v[0][2]};
        lds_signal(C_VB);
      }
      lds_wait(C_VB, 4);
      lds_wait(C_B, 4);
      u32x4 Q[4];
#pragma unroll
      for (int kk = 0; kk < 4; ++kk) Q[kk] = lds128(L_VB + (c & 7) * 272 + 64 * kk + 16 * g);
      float s[16][4]; float mx = -INFINITY;
#pragma unroll
      for (int blk = 0; blk < 16; ++blk) {
        f32x4 d = {0.f, 0.f, 0.f, 0.f};
#pragma unroll
        for (int kk = 0; kk < 4; ++kk) d = mfma16(lds128(L_R2 + (16 * blk + c) * 256 + 16 * ((4 * kk + g) ^ ((c & 3) << 2))), Q[kk], d);
#pragma unroll
        for (int r = 0; r < 4; ++r) { s[blk][r] = bf16r(bf16r(d[r]) * SCALE); mx = fmaxf(mx, s[blk][r]); }
      }
      const float mL = mulL(x32_max(x16_max(mx)));
      float l = 0.f;
#pragma unroll
      for (int blk = 0; blk < 16; ++blk)
#pragma unroll
        for (int r = 0; r < 4; ++r) { s[blk][r] = ex2(__builtin_fmaf(s[blk][r], LOG2E, -mL)); l += s[blk][r]; }
      l = x32_sum(x16_sum(l));
      f32x4 o0 = {0.f, 0.f, 0.f, 0.f}, o1 = {0.f, 0.f, 0.f, 0.f};
      const int q = (lane >> 2) & 3, pp = lane & 3;
      const unsigned va0 = L_R0 + (4 * g + q) * 256 + 16 * ((4 * w + (pp >> 1)) ^ (q << 2)) + 8 * (pp & 1);
#pragma unroll
      for (int tt = 0; tt < 8; ++tt) {
        const u32x4 P = {pack2(s[2 * tt][0], s[2 * tt][1]), pack2(s[2 * tt][2], s[2 * tt][3]), pack2(s[2 * tt + 1][0], s[2 * tt + 1][1]), pack2(s[2 * tt + 1][2], s[2 * tt + 1][3])};
        const u32x2 a0 = lds_tr(va0 + tt * 8192), a1 = lds_tr(va0 + tt * 8192 + 4096), b0 = lds_tr(va0 + tt * 8192 + 32), b1 = lds_tr(va0 + tt * 8192 + 4096 + 32);
        o0 = mfma16(u32x4{a0[0], a0[1], a1[0], a1[1]}, P, o0);
        o1 = mfma16(u32x4{b0[0], b0[1], b1[0], b1[1]}, P, o1);
      }
      asm volatile("global_store_dwordx4 %0, %1, %2 sc1 nt\n\ts_waitcnt vmcnt(0)" :: "v"(WS_PB + idx * PB_REC + w * 1024 + (c & 7) * 128 + (c >> 3) * 64 + g * 16), "v"(asu(c < 8 ? o0 : o1)), "s"(p.ws) : "memory");
      if (lds_ticket(C_PB) == 3) put2<BSN>(p.ws, WS_PB + idx * PB_REC + 4096 + (lane & 7) * 16, mL, l, tag, 0xffull);
    } else {
      if (job == 0) {
        u32x4 v[2] = {{0u, 0u, 0u, 0u}, {0u, 0u, 0u, 0u}};
        asm volatile("global_load_dwordx4 %0, %2, %3\n\tglobal_load_dwordx4 %1, %2, %3 offset:1024\n\ts_waitcnt vmcnt(0)" : "+v"(v[0]), "+v"(v[1]) : "v"(lane * 16), "s"(p.xB + w * DM) : "memory");
        *(LAS u32x4*)(L_VB + w * 2112 + lane * 16) = v[0]; *(LAS u32x4*)(L_VB + w * 2112 + 1024 + lane * 16) = v[1];
        lds_signal(C_VB);
        lds_wait(C_VB, 4);
        lds_wait(C_B, 4);
        *(LAS u16*)(L_OA + (g * 64 + 16 * w + c) * 2) = f2bf(sel4(piece4(lane, w), g));
        if (lds_ticket(C_AGG2) == 3) { const f32x2 e = lds64f(L_OA + lane * 8); put2<BSN>(p.ws, WS_QB + qd * 1024 + lane * 16, e[0], e[1], tag, ~0ull); }
      }
      if (w == 3) {
        nap(NAP_M);
        const unsigned rec = WS_PB + ((idx & ~7) + (lane >> 5)) * PB_REC;
        u32x4 ml[1]; const unsigned off[1] = {rec + 2 * (lane & 3) * PB_REC + 4096 + (idx & 7) * 16};
        gather<1, 64, BLN>(ml, p.ws, off, tag);
        u32x4 v[4];
        const unsigned po = rec + ((lane >> 3) & 3) * 1024 + (idx & 7) * 128 + (lane & 7) * 16;
        asm volatile("global_load_dwordx4 %0, %4, %8 sc1 nt\n\tglobal_load_dwordx4 %1, %5, %8 sc1 nt\n\tglobal_load_dwordx4 %2, %6, %8 sc1 nt\n\tglobal_load_dwordx4 %3, %7, %8 sc1 nt\n\ts_waitcnt vmcnt(0)"
                     : "=&v"(v[0]), "=&v"(v[1]), "=&v"(v[2]), "=&v"(v[3]) : "v"(po), "v"(po + 2 * PB_REC), "v"(po + 4 * PB_REC), "v"(po + 6 * PB_REC), "s"(p.ws) : "memory");
        const float m = dec(ml[0][0]);
        float M = fmaxf(m, dppf<XOR1>(m)); M = fmaxf(M, dppf<XOR2>(M)); M = x32_max(M);
        const float wt = ex2(m - M);
        float L = wt * dec(ml[0][2]); L += dppf<XOR1>(L); L += dppf<XOR2>(L); L = x32_sum(L);
        f32x4 a = asf(v[0]) * dppf<0x00>(wt) + asf(v[1]) * dppf<0x55>(wt) + asf(v[2]) * dppf<0xAA>(wt) + asf(v[3]) * dppf<0xFF>(wt);
#pragma unroll
        for (int r = 0; r < 4; ++r) a[r] = x32_sum(a[r]) / L;
        put2<BSN>(p.ws, WS_OB + ((((idx >> 3) & 3) * 4 + (idx >> 5)) * 8 + (idx & 7)) * 512 + (lane & 31) * 16, dec(pack2(a[0], a[1])), dec(pack2(a[2], a[3])), tag, 0xffffffffull);
      }
      if (job == 1) {
        nap(w == 3 ? NAP_O - NAP_M : NAP_O);
        u32x4 v[4]; unsigned off[4];
#pragma unroll
        for (int i = 0; i < 4; ++i) off[i] = WS_OB + ((qd >> 4) * 4 + w) * 4096 + i * 1024 + lane * 16;
        gather<4, 64, BLN>(v, p.ws, off, tag);
#pragma unroll
        for (int i = 0; i < 4; ++i) *(LAS u32x2*)(L_VB + w * 2112 + i * 512 + lane * 8) = u32x2{v[i][0], v[i][2]};
        lds_signal(C_VB);
        lds_wait(C_VB, 4);
        lds_wait(C_B, 4);
        const f32x4 d = piece4(lane, w);
        if (g < 2) *(LAS f32x2*)(L_OA + w * 256 + (lane & 31) * 8) = g ? f32x2{d[2], d[3]} : f32x2{d[0], d[1]};
        publish256<BSN>(p.ws, WS_YB + qd * 2048, C_AGG2, lane, tag);
        if (w == 0) {
          nap(2);
          u32x4 y[2];
          const unsigned base = WS_YB + ((lane >> 5) * 16 + (qd & 15)) * 2048 + (qd >> 4) * 512 + (lane & 31) * 16, yo[2] = {base, base + 32 * 2048};
          gather<2, 64, BLN>(y, p.ws, yo, tag);
          const float a = x32_sum(dec(y[0][0]) + dec(y[1][0])), b = x32_sum(dec(y[0][2]) + dec(y[1][2]));
          store_bf16(p.out, ((2 * (g & 1) + (g >> 1)) * 2 * DM + DM + 64 * (qd & 15) + 16 * (qd >> 4) + c) * 2, g < 2 ? a : b);
        }
      }
    }
    if (w == 1) STAMP(5);
    if (VT != 0 && w == VTW) {
      unsigned dum;
      asm volatile("global_load_dword %0, %1, %2 nt\n\ts_waitcnt vmcnt(0)" : "=&v"(dum) : "v"((lane & (VT - 1)) * (1048576 / (VT ? VT : 1))), "s"((const char*)p.vA + (size_t)ja * 1048576) : "memory");
    }
    lds_wait(C_KEND, 4);
    {
      const float mL = *(const LAS float*)(L_PART + w * 8);
      float l = 0.f;
#pragma unroll
      for (int i = 0; i < 2; ++i) {
        const u32x4 sc = lds128(L_SC + w * 2048 + i * 1024 + lane * 16);
#pragma unroll
        for (int k = 0; k < 4; ++k) l += ex2(__builtin_fmaf(lo16(sc[k]), LOG2E, -mL)) + ex2(__builtin_fmaf(hi16(sc[k]), LOG2E, -mL));
      }
      l += dppf<XOR1>(l); l += dppf<XOR2>(l); l += dppf<ROR4>(l); l += dppf<ROR8>(l); l = x32_sum(x16_sum(l));
      *(LAS float*)(L_PART + w * 8 + 4) = l;
      lds_signal(C_ML);
    }
    f32x4 fw = {0.f, 0.f, 0.f, 0.f};
    if (w == 0) {
      lds_wait(C_ML, 4);
      const f32x4 m01 = asf(lds128(L_PART)), m23 = asf(lds128(L_PART + 16));
      const float M = fmaxf(fmaxf(m01[0], m01[2]), fmaxf(m23[0], m23[2]));
      const float L = (ex2(m01[0] - M) * m01[1] + ex2(m01[2] - M) * m01[3]) + (ex2(m23[0] - M) * m23[1] + ex2(m23[2] - M) * m23[3]);
      put2(p.ws, WS_ML + ja * 128 + (lane & 7) * 16, M, L, tag, 0xffull);
      nap(3);
      u32x4 o[1]; const unsigned oo[1] = {WS_ML + (ja ^ 1) * 128 + (lane & 7) * 16};
      gather<1, 64>(o, p.ws, oo, tag);
      const float MM = fmaxf(M, dec(o[0][0])), LL = ex2(M - MM) * L + ex2(dec(o[0][0]) - MM) * dec(o[0][2]);
      fw = f32x4{ex2(m01[0] - MM), ex2(m01[2] - MM), ex2(m23[0] - MM), ex2(m23[2] - MM)} / LL;
    }
    pin(fw);
    unsigned off[4], yo[2], pa = WS_PA + (8 * ((ja >> 1) & 31) + 2 * (ja >> 6) + (ja & 1)) * PA_REC + lane * 16;
#pragma unroll
    for (int i = 0; i < 4; ++i) { off[i] = WS_PA + ((jo & ~15) + 8 * (i >> 1) + 2 * w + (i & 1)) * PA_REC + lane * 16; pin(off[i]); }
    const int n = 64 * (jo & 15) + 16 * w + c;
    unsigned ya = WS_YA + ((g & 1) * 128 + (n >> 3)) * 2048 + (jo >> 4) * 128 + (n & 7) * 16;
    unsigned oa = ((2 * (j >> 7) + ((lane >> 3) & 1)) * 2 * DM + 8 * (j & 127) + (lane & 7)) * 2;
    unsigned lp = L_PART + 64 + 8 * lane, lw = L_OA + w * 576 + 4 * lane, lr = L_OA + (lane & 3) * 576 + 16 * g;
    yo[0] = WS_YA + j * 2048 + lane * 16; yo[1] = yo[0] + 1024;
    pin(pa); pin(ya); pin(oa); pin(lp); pin(lw); pin(lr); pin(yo[0]); pin(yo[1]);
    if (TOUCH != 0) {
      u32x4 dum;
      if (w != 0) asm volatile("global_load_dwordx4 %0, %1, %2 sc1\n\ts_waitcnt vmcnt(0)" : "=&v"(dum) : "v"(w < 2 ? pa : yo[w & 1]), "s"(p.ws) : "memory");
    }
    waitvm<0>();
    wg_barrier();
    if (w == 0) {
      STAMP(6);
      const f32x2 s = (fw[0] * lds64f(lp) + fw[1] * lds64f(lp + 512)) + (fw[2] * lds64f(lp + 1024) + fw[3] * lds64f(lp + 1536));
      put2(p.ws, pa, s[0], s[1], tag, ~0ull);
      STAMP(19);
    }
    u32x4 v[4];
    if (PA_NAP != 0) __builtin_amdgcn_s_sleep(PA_NAP);
    gather<4, PA_SLEEP>(v, p.ws, off, tag);
    STAMP(20 + w);
    *(LAS unsigned*)lw = pack2(dec(v[0][0]) + dec(v[1][0]), dec(v[0][2]) + dec(v[1][2]));
    *(LAS unsigned*)(lw + 256) = pack2(dec(v[2][0]) + dec(v[3][0]), dec(v[2][2]) + dec(v[3][2]));
    wg_barrier();
    const f32x4 d = piece(lr, F, f32x4{0.f, 0.f, 0.f, 0.f});
    put2(p.ws, ya, g ? d[2] : d[0], g ? d[3] : d[1], tag, 0xffffffffull);
    if (w == 0) {
      STAMP(8);
      u32x4 y[2];
      if (Y_NAP != 0) __builtin_amdgcn_s_sleep(Y_NAP);
      gather<2, Y_SLEEP>(y, p.ws, yo, tag);
      const float a = dec(y[0][0]) + dec(y[1][0]), b = dec(y[0][2]) + dec(y[1][2]);
      const float k = (lane & 8) ? b : a, sd = (lane & 8) ? a : b;
      const float r = x32_sum(x16_sum(k + dppf<ROR8>(sd)));
      if (lane < 16) store_bf16(p.out, oa, r);
      STAMP(9);
      if (j == 0 && lane == 0) __hip_atomic_store((GAS u64*)(p.ws + WS_EPOCH), epoch + 1, __ATOMIC_RELAXED, __HIP_MEMORY_SCOPE_AGENT);
    }
    return;
  }

  if (wave == 0) STAMP(0);
  const unsigned vq0 = wtile_voff(lane, 0, HD * 2), vq1 = wtile_voff(lane, 1, HD * 2);
  const size_t qoff = ((size_t)((j & 3) * 256 + wave * 64) * HD + (jq >> 2) * 64) * 2;
  u32x4 R[8];
  static_for<0, 8>([&](auto ic) __attribute__((always_inline)) {
    constexpr int i = decltype(ic)::value;
    u32x4& r = R[i];
    asm volatile("" : "=v"(r));
    aload<0>(r, (i & 1) ? vq1 : vq0, (const char*)p.wqA + qoff + (size_t)i * 8 * HD * 2);
  });
  unsigned t0 = (unsigned)__builtin_amdgcn_s_memrealtime();
  if (PRIO != 0) __builtin_amdgcn_s_setprio(PRIO);
  int jb = job; asm volatile("" : "+s"(jb));
  const unsigned vo0 = wtile_voff(lane, 0, DM * 2), vo1 = wtile_voff(lane, 1, DM * 2), vkv = g * 256 + 16 * (c ^ (g << 2));
  const size_t ooff = ((size_t)((jo >> 4) * 256 + wave * 64) * DM + (jo & 15) * 64) * 2;
  const unsigned vb0 = jb == 0 ? vq0 : jb == 1 ? vo0 : vkv, vb1 = jb == 0 ? vq1 : jb == 1 ? vo1 : vkv;
  unsigned bstep = jb == 0 ? 8 * HD * 2 : jb == 1 ? 8 * DM * 2 : 1024;
  asm volatile("" : "+s"(bstep));
  const unsigned boff = (jb == 0 ? qd * 128 : jb == 1 ? ((qd >> 4) * 1024 * DM + (qd & 15) * 64) * 2
                                                      : ((idx >> 3) * SK + (SK - WIN) + (idx & 7) * 256) * DH * 2) + wave * 8 * bstep;
  const char* bp = (const char*)(jb == 0 ? p.wqB : jb == 1 ? p.woB : p.kB) + boff;
  const char* const b23 = jb < 2 ? bp + 64 * bstep : (const char*)p.vB + boff;
  const unsigned voffV = lane * 16, voffK = KLIN ? lane * 16 : ((lane >> 2) & 3) * 256 + g * 64 + (lane & 3) * 16;
  const unsigned ldst = wave * 8192 + lane * 16;
  const char* const kp = (const char*)p.kA + (size_t)ja * 1048576 + wave * 8192 + 4096;
  const char* const vp = (const char*)p.vA + (size_t)ja * 1048576 + wave * 8192 + 4096;
  static_for<8, 56>([&](auto nc) __attribute__((always_inline)) {
    constexpr int n = decltype(nc)::value, k = n >> 3, i = n & 7;
    constexpr unsigned reg = ((k - 1) & 3) * 32768;
    if constexpr (n == 40) lds_wait(C_FREE0, 4);
    if constexpr (n == 48) lds_wait(C_FREE1, 4);
    stage<n < 16 ? PW0 : PW, (reg & 0xffffu) + i * 1024>(R[i], ldst + (reg & ~0xffffu));
    if constexpr (k == 1) aload<0>(R[i], (i & 1) ? vo1 : vo0, (const char*)p.woA + ooff + (size_t)i * 8 * DM * 2);
    if constexpr (k >= 2 && k <= 5) {
      if constexpr (n == 32) bp = b23;
      aload<0>(R[i], (i & 1) ? vb1 : vb0, bp);
      bp += (i == 7 ? 25 : 1) * bstep;
    }
    if constexpr (k == 6) aload<1024 * i - 4096>(R[i], voffK, kp);
    if constexpr (i == 7 && k < 6) { if (wave == 0) STAMP(23 + k); }
    if constexpr (n == 15) wg_barrier();
    if constexpr (n == 23) lds_signal(C_WOA);
    if constexpr (n == 55) lds_signal(C_B);
  });
  if (wave == 0) STAMP(1);
  lds_wait(C_QA, 1);
  if (wave == 0) STAMP(2);
  if (TBASE != 0) t0 = __builtin_amdgcn_readfirstlane(*(const LAS unsigned*)(L_CNT + 4 * C_T0));
  unsigned due = (t0 + SCH0) * 16u; int prev = 1 << 20;
  const unsigned sca = L_SC + wave * 2048 + (lane >> 2) * 4;
  float mx = -INFINITY;
  {
    u32x4 QB[4];
    const u32x4 qv = lds128(L_QA + 16 * (4 * g + (lane & 3)));
#pragma unroll
    for (int i = 0; i < 4; ++i) QB[i] = ((c >> 2) == i) ? qv : u32x4{0u, 0u, 0u, 0u};
#pragma unroll 1
    for (unsigned r = 1; r <= 32; ++r) {
      if (wave == 0 && (r & 7) == 0) STAMP(10 + (r >> 3));
      const unsigned sc = score_step(R, QB, r == 32 ? voffV : voffK, r == 32 ? vp : kp + r * 32768, pace_depth(due, prev, SCH));
      *(LAS unsigned*)(sca + (r - 1) * 64) = sc;
      mx = fmaxf(mx, fmaxf(lo16(sc), hi16(sc)));
    }
  }
  mx = fmaxf(mx, dppf<ROR4>(mx)); mx = fmaxf(mx, dppf<ROR8>(mx));
  const float mL = mulL(x32_max(x16_max(mx)));
  *(LAS float*)(L_PART + wave * 8) = mL;
  lds_signal(C_KEND);
  if (wave == 0) STAMP(4);
  unsigned msk[4];
#pragma unroll
  for (int i = 0; i < 4; ++i) msk[i] = (c == 2 * i) ? 0x0000ffffu : (c == 2 * i + 1) ? 0xffff0000u : 0u;
  f32x4 acc = {0.f, 0.f, 0.f, 0.f};
#pragma unroll 1
  for (unsigned r = 1; r < 32; ++r) {
    if (wave == 0 && (r & 7) == 0) STAMP(14 + (r >> 3));
    value_step<true>(acc, mL, *(const LAS unsigned*)(sca + (r - 1) * 64), R, msk, voffV, vp + r * 32768, pace_depth(due, prev, SCHV));
  }
  unsigned pw = L_PART + 64 + wave * 512 + (32 * g + c) * 4; pin(pw);
  value_step<false>(acc, mL, *(const LAS unsigned*)(sca + 31 * 64), R, msk, 0u, nullptr, 0u);
  if (c < 8) {
#pragma unroll
    for (int r = 0; r < 4; ++r) *(LAS float*)(pw + 32 * r) = acc[r];
  }
  wg_barrier();
}

extern "C" void kernel_launch(void* const* d_in, const int* in_sizes, int n_in, void* d_out, int out_size, void* d_ws, size_t ws_size, hipStream_t stream) {
  Params p;
  p.xA = (const u16*)d_in[0]; p.kA = (const u16*)d_in[1]; p.vA = (const u16*)d_in[2]; p.wqA = (const u16*)d_in[3]; p.woA = (const u16*)d_in[4];
  p.xB = (const u16*)d_in[5]; p.kB = (const u16*)d_in[6]; p.vB = (const u16*)d_in[7]; p.wqB = (const u16*)d_in[8]; p.woB = (const u16*)d_in[9];
  p.out = (u16*)d_out; p.ws = (unsigned char*)d_ws;
  (void)hipFuncSetAttribute((const void*)fused_decode, hipFuncAttributeMaxDynamicSharedMemorySize, LDS_BYTES);
  fused_decode<<<NWG, NTHR, LDS_BYTES, stream>>>(p);
}
```

```cpp
#include <hip/hip_runtime.h>
#include <hip/hip_bf16.h>
#include <cstdint>
#pragma clang diagnostic ignored "-Wint-to-pointer-cast"

typedef __attribute__((ext_vector_type(4))) unsigned u32x4;
typedef __attribute__((ext_vector_type(2))) unsigned u32x2;
typedef __attribute__((ext_vector_type(4))) float f32x4;
typedef __attribute__((ext_vector_type(2))) float f32x2;
typedef __attribute__((ext_vector_type(8))) __bf16 bf16x8;
typedef __attribute__((ext_vector_type(2))) __bf16 bf16x2;
typedef __attribute__((ext_vector_type(4))) short s16x4;
typedef unsigned short u16;
typedef unsigned long long u64;
#define LAS __attribute__((address_space(3)))
#define GAS __attribute__((address_space(1)))
#define INL __device__ __forceinline__

constexpr int DM = 1024, DH = 128, SK = 8192, WIN = 2048, HD = 4096;
constexpr float SCALE = 0.088388347648318440f, LOG2E = 1.4426950408889634f;
constexpr int NWG = 256, NTHR = 512;

#define SCH_START 760
#define SCH_EARLY 1750
#define SCH_LATE 1700
constexpr int SCH_KNEE = 8, SCH_JUMP = 150;
constexpr int NAP_Q = 6, NAP_M = 12, NAP_O = 18, SLEEP_PA = 16, SLEEP_Y = 12;

constexpr unsigned WS_EPOCH = 0;
constexpr unsigned WS_T0 = 4096;
constexpr unsigned WS_QA = WS_T0 + NWG * 16;
constexpr unsigned WS_PA = WS_QA + NWG * 2048;
constexpr unsigned WS_ML = WS_PA + NWG * 1024;
constexpr unsigned WS_YA = WS_ML + NWG * 128;
constexpr unsigned WS_QB = WS_YA + NWG * 2048;
constexpr unsigned PB_REC = 4096 + 128;
constexpr unsigned WS_PB = WS_QB + 64 * 1024;
constexpr unsigned WS_OB = WS_PB + 128 * PB_REC;
constexpr unsigned WS_YB = WS_OB + 128 * 512;
constexpr unsigned WS_END = WS_YB + 64 * 2048;
static_assert(WS_END <= (4u << 20), "workspace");

constexpr unsigned L_R0 = 0, L_R1 = 32768, L_R2 = 65536;
constexpr unsigned L_QA = 131072;
constexpr unsigned L_OA = L_QA + 256;
constexpr unsigned L_PART = L_OA + 4 * 576;
constexpr unsigned L_XA = L_PART + 64 + 4 * 512;
constexpr unsigned L_VB = L_XA + 2048;
constexpr unsigned L_SC = L_VB + 4 * 2112;
constexpr unsigned L_CNT = L_SC + 8192;
constexpr unsigned LDS_BYTES = L_CNT + 64;
enum { C_WOA = 0, C_B, C_FREE0, C_FREE1, C_QA, C_VB, C_PB, C_AGG, C_AGG2, C_T0, C_KEND, C_ML };
constexpr unsigned SPIN_CAP = 1u << 17;

struct Params {
  const u16 *wqA, *xA, *kA, *vA, *woA, *xB, *kB, *vB, *wqB, *woB;
  u16* out;
  unsigned char* ws;
};

template <int V> struct IC { static constexpr int value = V; };
template <int A, int B, typename F> INL void static_for(F&& f) { if constexpr (A < B) { f(IC<A>{}); static_for<A + 1, B>(f); } }
INL u16 f2bf(float f) { return __builtin_bit_cast(u16, (__bf16)f); }
INL float bf16r(float f) { return __uint_as_float(((unsigned)f2bf(f)) << 16); }
INL unsigned pack2(float lo, float hi) { return __builtin_bit_cast(unsigned, __builtin_convertvector(f32x2{lo, hi}, bf16x2)); }
INL float lo16(unsigned u) { return __uint_as_float(u << 16); }
INL float hi16(unsigned u) { return __uint_as_float(u & 0xffff0000u); }
template <int CTRL> INL float dppf(float x) { return __builtin_bit_cast(float, __builtin_amdgcn_mov_dpp(__builtin_bit_cast(int, x), CTRL, 0xf, 0xf, true)); }
template <int CTRL> INL unsigned dppu(unsigned x) { return (unsigned)__builtin_amdgcn_mov_dpp((int)x, CTRL, 0xf, 0xf, true); }
constexpr int XOR1 = 0xB1, XOR2 = 0x4E, HMIR = 0x141, ROR4 = 0x124, ROR8 = 0x128, BCAST = 0x150;
INL float x16_max(float x) { auto s = __builtin_amdgcn_permlane16_swap(__float_as_uint(x), __float_as_uint(x), false, false); return fmaxf(__uint_as_float(s[0]), __uint_as_float(s[1])); }
INL float x32_max(float x) { auto s = __builtin_amdgcn_permlane32_swap(__float_as_uint(x), __float_as_uint(x), false, false); return fmaxf(__uint_as_float(s[0]), __uint_as_float(s[1])); }
INL float x16_sum(float x) { auto s = __builtin_amdgcn_permlane16_swap(__float_as_uint(x), __float_as_uint(x), false, false); return __uint_as_float(s[0]) + __uint_as_float(s[1]); }
INL float x32_sum(float x) { auto s = __builtin_amdgcn_permlane32_swap(__float_as_uint(x), __float_as_uint(x), false, false); return __uint_as_float(s[0]) + __uint_as_float(s[1]); }
INL f32x4 mfma16(u32x4 a, u32x4 b, f32x4 c) { return __builtin_amdgcn_mfma_f32_16x16x32_bf16(__builtin_bit_cast(bf16x8, a), __builtin_bit_cast(bf16x8, b), c, 0, 0, 0); }
INL float ex2(float x) { return __builtin_amdgcn_exp2f(x); }
INL float mulL(float m) { float r = m * LOG2E; asm volatile("" : "+v"(r)); return r; }
INL float prob(float s, float mL) { return ex2(__builtin_fmaf(s, LOG2E, -mL)); }
INL u32x4 asu(const f32x4& v) { return __builtin_bit_cast(u32x4, v); }
INL f32x4 asf(const u32x4& v) { return __builtin_bit_cast(f32x4, v); }
INL float dec(unsigned v) { return __uint_as_float(v); }
INL float sel4(const f32x4& d, int g) { return (g & 2) ? ((g & 1) ? d[3] : d[2]) : ((g & 1) ? d[1] : d[0]); }
template <typename T> INL void pin(T& d) { asm volatile("" : "+v"(d)); }
INL unsigned clock10ns() { return (unsigned)__builtin_amdgcn_s_memrealtime(); }

INL u32x4 lds128(unsigned a) { return *(const LAS u32x4*)a; }
INL f32x2 lds64f(unsigned a) { return *(const LAS f32x2*)a; }
INL u32x2 lds_tr(unsigned a) { return __builtin_bit_cast(u32x2, __builtin_amdgcn_ds_read_tr16_b64_v4i16((LAS s16x4*)a)); }
INL void lds_signal(int c) {
  u64 keep;
  asm volatile("s_waitcnt lgkmcnt(0)\n\ts_mov_b64 %0, exec\n\ts_mov_b64 exec, 1\n\tds_add_u32 %1, %2\n\ts_mov_b64 exec, %0" : "=&s"(keep) : "v"(L_CNT + 4 * c), "v"(1u) : "memory");
}
INL unsigned lds_ticket(int c) {
  unsigned old; u64 keep;
  asm volatile("s_waitcnt lgkmcnt(0)\n\ts_mov_b64 %1, exec\n\ts_mov_b64 exec, 1\n\tds_add_rtn_u32 %0, %2, %3\n\ts_mov_b64 exec, %1\n\ts_waitcnt lgkmcnt(0)"
               : "=&v"(old), "=&s"(keep) : "v"(L_CNT + 4 * c), "v"(1u) : "memory");
  return __builtin_amdgcn_readfirstlane(old);
}
INL void lds_wait(int c, unsigned n) {
#pragma unroll 1
  for (unsigned s = 0; s < SPIN_CAP * 8; ++s) {
    if ((unsigned)__builtin_amdgcn_readfirstlane(*(volatile LAS unsigned*)(L_CNT + 4 * c)) >= n) break;
    __builtin_amdgcn_s_sleep(1);
  }
  asm volatile("" ::: "memory");
}
INL void wg_barrier() { asm volatile("s_waitcnt lgkmcnt(0)\n\ts_barrier" ::: "memory"); }
INL void nap(int n) {
#pragma unroll 1
  for (int i = 0; i < n; ++i) __builtin_amdgcn_s_sleep(64);
}

template <int OFF> INL void aload(u32x4& d, unsigned voff, const void* sbase) {
  asm volatile("global_load_dwordx4 %0, %1, %2 offset:%3 nt" : "+v"(d) : "v"(voff), "s"(sbase), "i"(OFF) : "memory");
}
template <int I> INL void sload(u32x4& d, unsigned voff, const char* base) {
  aload<(I & 1) * 4096 - 4096>(d, voff, base + (I >> 1) * 8192);
}
template <int N> INL void waitv(u32x4& d) { asm volatile("s_waitcnt vmcnt(%1)" : "+v"(d) : "i"(N) : "memory"); }
template <int N> INL void waitvm() { asm volatile("s_waitcnt vmcnt(%0)" :: "i"(N) : "memory"); }
INL void pacev(u32x4& d, unsigned depth) {
  asm volatile("s_cmp_eq_u32 %1, 7\n\ts_cbranch_scc1 7f\n\ts_cmp_eq_u32 %1, 6\n\ts_cbranch_scc1 6f\n\ts_cmp_eq_u32 %1, 5\n\ts_cbranch_scc1 5f\n\t"
               "s_waitcnt vmcnt(4)\n5:\n\ts_waitcnt vmcnt(5)\n6:\n\ts_waitcnt vmcnt(6)\n7:\n\ts_waitcnt vmcnt(7)" : "+v"(d) : "s"(depth) : "memory", "scc");
}
template <int W, int OFF> INL void stage(u32x4& d, unsigned lds_addr) {
  asm volatile("s_waitcnt vmcnt(%2)\n\tds_write_b128 %1, %0 offset:%3" : "+v"(d) : "v"(lds_addr), "i"(W), "i"(OFF) : "memory");
}
INL unsigned pace_depth(unsigned& due, int& prev, unsigned step) {
  due += step;
  int late = (int)(clock10ns() * 16u - due) >> 4;
  if (late - prev > SCH_JUMP) { due += (unsigned)(late - prev) * 16u; late = prev; }
  prev = late;
  return __builtin_amdgcn_readfirstlane(min(max((late >> 6) + 5, 4), 7));
}

template <bool NT = false> INL void put2(const unsigned char* ws, unsigned off, float a, float b, unsigned tag, u64 mask) {
  u64 keep;
  const u32x4 v = {__float_as_uint(a), tag, __float_as_uint(b), tag};
  if (NT) asm volatile("s_mov_b64 %0, exec\n\ts_mov_b64 exec, %4\n\tglobal_store_dwordx4 %1, %2, %3 sc1 nt\n\ts_mov_b64 exec, %0\n\ts_nop 0"
                       : "=&s"(keep) : "v"(off), "v"(v), "s"(ws), "s"(mask) : "memory");
  else asm volatile("s_mov_b64 %0, exec\n\ts_mov_b64 exec, %4\n\tglobal_store_dwordx4 %1, %2, %3 sc1\n\ts_mov_b64 exec, %0\n\ts_nop 0"
                    : "=&s"(keep) : "v"(off), "v"(v), "s"(ws), "s"(mask) : "memory");
}
INL void poll16(u32x4& d, const unsigned char* ws, unsigned off, unsigned need) {
  asm volatile("s_cmp_eq_u32 %3, 0\n\ts_cbranch_scc1 1f\n\tglobal_load_dwordx4 %0, %1, %2 sc1\n1:" : "+v"(d) : "v"(off), "s"(ws), "s"(need) : "memory", "scc");
}
template <int N, int SLEEP> INL void gather(u32x4 (&v)[N], const unsigned char* ws, const unsigned (&off)[N], unsigned tag) {
  unsigned need[N];
#pragma unroll
  for (int i = 0; i < N; ++i) { asm volatile("" : "=v"(v[i])); need[i] = 1; }
#pragma unroll 1
  for (unsigned s = 0; s < SPIN_CAP; ++s) {
#pragma unroll
    for (int i = 0; i < N; ++i) poll16(v[i], ws, off[i], need[i]);
    waitvm<0>();
    unsigned bad[N], any = 0;
#pragma unroll
    for (int i = 0; i < N; ++i) pin(v[i]);
#pragma unroll
    for (int i = 0; i < N; ++i) { bad[i] = (v[i][1] ^ tag) | (v[i][3] ^ tag); any |= bad[i]; }
    pin(any);
    if (__builtin_amdgcn_ballot_w64(any != 0) == 0) break;
#pragma unroll
    for (int i = 0; i < N; ++i) { const u64 m = __builtin_amdgcn_ballot_w64(bad[i] != 0); need[i] = (unsigned)m | (unsigned)(m >> 32); }
    if (SLEEP) __builtin_amdgcn_s_sleep(SLEEP);
  }
}
template <bool NT> INL void publish256(const unsigned char* ws, unsigned off, int cnt, int lane, unsigned tag) {
  if (lds_ticket(cnt) == 3) {
    const f32x2 e0 = lds64f(L_OA + lane * 8), e1 = lds64f(L_OA + 512 + lane * 8);
    put2<NT>(ws, off + lane * 16, e0[0], e0[1], tag, ~0ull);
    put2<NT>(ws, off + 1024 + lane * 16, e1[0], e1[1], tag, ~0ull);
  }
}
INL void store_bf16(u16* out, unsigned byte_off, float v) { asm volatile("global_store_short %0, %1, %2 sc1" :: "v"(byte_off), "v"(pack2(v, v)), "s"(out) : "memory"); }

INL unsigned score_step(u32x4 (&R)[8], const u32x4 (&QB)[4], unsigned voff, const char* next, unsigned depth) {
  f32x4 s[2] = {{0.f, 0.f, 0.f, 0.f}, {0.f, 0.f, 0.f, 0.f}};
  static_for<0, 8>([&](auto ic) __attribute__((always_inline)) {
    constexpr int i = decltype(ic)::value;
    pacev(R[i], depth);
    s[i >> 2] = mfma16(R[i], QB[i & 3], s[i >> 2]);
    __builtin_amdgcn_sched_barrier(0);
    sload<i>(R[i], voff, next);
    __builtin_amdgcn_sched_barrier(0);
  });
  float d = dppf<0x00>(s[0][0]); d += dppf<0x55>(s[0][1]); d += dppf<0xAA>(s[0][2]); d += dppf<0xFF>(s[0][3]);
  float e = dppf<0x00>(s[1][0]); e += dppf<0x55>(s[1][1]); e += dppf<0xAA>(s[1][2]); e += dppf<0xFF>(s[1][3]);
  const unsigned u = pack2(d, e);
  return pack2(lo16(u) * SCALE, hi16(u) * SCALE);
}
template <bool MORE>
INL void value_step(f32x4& acc, float mL, unsigned sc, u32x4 (&R)[8], const unsigned (&msk)[4], unsigned voff, const char* next, unsigned depth) {
  const float pa = prob(lo16(sc), mL), pb = prob(hi16(sc), mL);
  unsigned pk[2] = {pack2(pa, pa), pack2(pb, pb)};
  pin(pk[0]); pin(pk[1]);
  static_for<0, 8>([&](auto ic) __attribute__((always_inline)) {
    constexpr int i = decltype(ic)::value;
    const unsigned b = pk[i >> 2];
    u32x4 P = {dppu<BCAST + 4 * (i & 3)>(b) & msk[0], dppu<BCAST + 4 * (i & 3)>(b) & msk[1], dppu<BCAST + 4 * (i & 3)>(b) & msk[2], dppu<BCAST + 4 * (i & 3)>(b) & msk[3]};
    if constexpr (MORE) pacev(R[i], depth); else { pin(P); waitv<7 - i>(R[i]); }
    acc = mfma16(R[i], P, acc);
    __builtin_amdgcn_sched_barrier(0);
    if constexpr (MORE) sload<i>(R[i], voff, next);
  });
}

INL void load_wfrag(u32x4 (&B)[8], unsigned region, int w, int lane) {
  const int g = lane >> 4, q = (lane >> 2) & 3, pp = lane & 3;
  const unsigned sw = 2u * ((q >> 1) | ((g & 1) << 1));
  const unsigned a = region + (8 * g + q) * 128 + 16 * ((2 * w + (pp >> 1)) ^ sw) + 8 * (pp & 1);
#pragma unroll
  for (int kk = 0; kk < 8; ++kk) { const u32x2 lo = lds_tr(a + kk * 4096), hi = lds_tr(a + kk * 4096 + 512); B[kk] = u32x4{lo[0], lo[1], hi[0], hi[1]}; }
}
INL f32x4 piece(unsigned a, const u32x4 (&B)[8], f32x4 d) {
  u32x4 A[8];
#pragma unroll
  for (int kk = 0; kk < 8; ++kk) A[kk] = lds128(a + 64 * kk);
  __builtin_amdgcn_sched_barrier(0);
#pragma unroll
  for (int kk = 0; kk < 8; ++kk) d = mfma16(A[kk], B[kk], d);
  return d;
}
INL f32x4 piece4(int lane, int w) {
  f32x4 d = {0.f, 0.f, 0.f, 0.f};
#pragma unroll 1
  for (int tt = 0; tt < 4; ++tt) { u32x4 B[8]; load_wfrag(B, ((tt + 2) & 3) * 32768, w, lane); d = piece(L_VB + tt * 512 + (lane & 3) * 2112 + 16 * (lane >> 4), B, d); }
  return d;
}
INL unsigned wtile_voff(int lane, int odd, unsigned rs) {
  const unsigned sw = 2u * (((lane >> 4) & 1) | (odd << 1));
  return (lane >> 3) * rs + 16 * ((lane & 7) ^ sw);
}

__global__ void __launch_bounds__(NTHR, 2) fused_decode(Params p) {
  const int lane = threadIdx.x & 63, wave = __builtin_amdgcn_readfirstlane(threadIdx.x >> 6);
  const int j = blockIdx.x, g = lane >> 4, c = lane & 15;
  const int ja = (j & ~15) | ((j & 7) << 1) | ((j >> 3) & 1), jq = j ^ 8, jo = ((j & 15) << 4) | (j >> 4);
  const int job = (j >> 3) & 3, qd = ((j & 7) << 3) | (j >> 5), idx = job >= 2 ? ((j & 7) << 4) | ((job & 1) << 3) | (j >> 5) : 2 * qd + job;

  if (wave >= 4) {
    const int w = wave - 4;
    const unsigned th = clock10ns();
    u32x4 xv = {0u, 0u, 0u, 0u};
    if (w < 2) asm volatile("global_load_dwordx4 %0, %1, %2" : "+v"(xv) : "v"((lane >> 5) * DM * 2 + (lane & 31) * 16), "s"((const char*)p.xA + (w * 2 * DM + (j & 3) * 256) * 2) : "memory");
    if (w == 3 && lane < 16) *(LAS unsigned*)(L_CNT + 4 * lane) = 0u;
    u64 epoch;
    asm volatile("s_load_dwordx2 %0, %1, 0x0\n\ts_waitcnt lgkmcnt(0)" : "=s"(epoch) : "s"(p.ws + WS_EPOCH) : "memory");
    u64 z = epoch + 0x9E3779B97F4A7C15ull;
    z = (z ^ (z >> 30)) * 0xBF58476D1CE4E5B9ull; z = (z ^ (z >> 27)) * 0x94D049BB133111EBull; z ^= z >> 31;
    const unsigned tag = (unsigned)(z >> 32);
    if (w == 2) put2(p.ws, WS_T0 + jq * 16, dec(th), dec(th), tag, 1ull);
    if (w < 2) { waitv<0>(xv); *(LAS u32x4*)(L_XA + w * 1024 + lane * 16) = xv; }
    wg_barrier();
    u32x4 F[8];
    load_wfrag(F, L_R0, w, lane);
    *(LAS float*)(L_OA + (g * 64 + 16 * w + c) * 4) = sel4(piece(L_XA + (lane & 3) * 512 + 16 * g, F, f32x4{0.f, 0.f, 0.f, 0.f}), g);
    publish256<false>(p.ws, WS_QA + jq * 2048, C_AGG, lane, tag);
    lds_signal(C_FREE0);
    lds_wait(C_WOA, 4);
    load_wfrag(F, L_R1, w, lane);
#pragma unroll
    for (int kk = 0; kk < 8; ++kk) pin(F[kk]);
    lds_signal(C_FREE1);
    if (w == 0) {
      u32x4 v[5]; unsigned off[5];
#pragma unroll
      for (int i = 0; i < 4; ++i) off[i] = WS_QA + (8 * ((ja >> 1) & 31) + 4 * (lane >> 5) + i) * 2048 + (ja >> 6) * 512 + (lane & 31) * 16;
      off[4] = WS_T0 + (8 * ((ja >> 1) & 31) + (lane & 7)) * 16;
      gather<5, 8>(v, p.ws, off, tag);
      unsigned dt = v[4][0] - th;
      dt += dppu<XOR1>(dt); dt += dppu<XOR2>(dt); dt += dppu<HMIR>(dt);
      *(LAS unsigned*)(L_CNT + 4 * C_T0) = th + (unsigned)((int)dt >> 3);
      *(LAS unsigned*)(L_QA + 4 * lane) = pack2((dec(v[0][0]) + dec(v[1][0])) + (dec(v[2][0]) + dec(v[3][0])), (dec(v[0][2]) + dec(v[1][2])) + (dec(v[2][2]) + dec(v[3][2])));
      lds_signal(C_QA);
    }
    if (job >= 2) {
      {
        nap(NAP_Q);
        u32x4 v[1]; const unsigned off[1] = {WS_QB + (((idx >> 3) & 3) * 16 + 4 * w + g) * 1024 + (idx >> 5) * 256 + c * 16};
        gather<1, 64>(v, p.ws, off, tag);
        *(LAS u32x2*)(L_VB + (2 * w + (lane >> 5)) * 272 + (lane & 31) * 8) = u32x2{v[0][0], v[0][2]};
        lds_signal(C_VB);
      }
      lds_wait(C_VB, 4);
      lds_wait(C_B, 4);
      u32x4 Q[4];
#pragma unroll
      for (int kk = 0; kk < 4; ++kk) Q[kk] = lds128(L_VB + (c & 7) * 272 + 64 * kk + 16 * g);
      float s[16][4]; float mx = -INFINITY;
#pragma unroll
      for (int blk = 0; blk < 16; ++blk) {
        f32x4 d = {0.f, 0.f, 0.f, 0.f};
#pragma unroll
        for (int kk = 0; kk < 4; ++kk) d = mfma16(lds128(L_R2 + (16 * blk + c) * 256 + 16 * ((4 * kk + g) ^ ((c & 3) << 2))), Q[kk], d);
#pragma unroll
        for (int r = 0; r < 4; ++r) { s[blk][r] = bf16r(bf16r(d[r]) * SCALE); mx = fmaxf(mx, s[blk][r]); }
      }
      const float mL = mulL(x32_max(x16_max(mx)));
      float l = 0.f;
#pragma unroll
      for (int blk = 0; blk < 16; ++blk)
#pragma unroll
        for (int r = 0; r < 4; ++r) { s[blk][r] = prob(s[blk][r], mL); l += s[blk][r]; }
      l = x32_sum(x16_sum(l));
      f32x4 o0 = {0.f, 0.f, 0.f, 0.f}, o1 = {0.f, 0.f, 0.f, 0.f};
      const int q = (lane >> 2) & 3, pp = lane & 3;
      const unsigned va0 = L_R0 + (4 * g + q) * 256 + 16 * ((4 * w + (pp >> 1)) ^ (q << 2)) + 8 * (pp & 1);
#pragma unroll
      for (int tt = 0; tt < 8; ++tt) {
        const u32x4 P = {pack2(s[2 * tt][0], s[2 * tt][1]), pack2(s[2 * tt][2], s[2 * tt][3]), pack2(s[2 * tt + 1][0], s[2 * tt + 1][1]), pack2(s[2 * tt + 1][2], s[2 * tt + 1][3])};
        const u32x2 a0 = lds_tr(va0 + tt * 8192), a1 = lds_tr(va0 + tt * 8192 + 4096), b0 = lds_tr(va0 + tt * 8192 + 32), b1 = lds_tr(va0 + tt * 8192 + 4096 + 32);
        o0 = mfma16(u32x4{a0[0], a0[1], a1[0], a1[1]}, P, o0);
        o1 = mfma16(u32x4{b0[0], b0[1], b1[0], b1[1]}, P, o1);
      }
      asm volatile("global_store_dwordx4 %0, %1, %2 sc1 nt\n\ts_waitcnt vmcnt(0)"
                   :: "v"(WS_PB + idx * PB_REC + w * 1024 + (c & 7) * 128 + (c >> 3) * 64 + g * 16), "v"(asu(c < 8 ? o0 : o1)), "s"(p.ws) : "memory");
      if (lds_ticket(C_PB) == 3) put2<true>(p.ws, WS_PB + idx * PB_REC + 4096 + (lane & 7) * 16, mL, l, tag, 0xffull);
    } else {
      if (job == 0) {
        u32x4 v0, v1;
        asm volatile("global_load_dwordx4 %0, %2, %3\n\tglobal_load_dwordx4 %1, %2, %3 offset:1024\n\ts_waitcnt vmcnt(0)" : "=&v"(v0), "=&v"(v1) : "v"(lane * 16), "s"(p.xB + w * DM) : "memory");
        *(LAS u32x4*)(L_VB + w * 2112 + lane * 16) = v0; *(LAS u32x4*)(L_VB + w * 2112 + 1024 + lane * 16) = v1;
        lds_signal(C_VB);
        lds_wait(C_VB, 4);
        lds_wait(C_B, 4);
        *(LAS u16*)(L_OA + (g * 64 + 16 * w + c) * 2) = f2bf(sel4(piece4(lane, w), g));
        if (lds_ticket(C_AGG2) == 3) { const f32x2 e = lds64f(L_OA + lane * 8); put2<true>(p.ws, WS_QB + qd * 1024 + lane * 16, e[0], e[1], tag, ~0ull); }
      }
      if (w == 3) {
        nap(NAP_M);
        const unsigned rec = WS_PB + ((idx & ~7) + (lane >> 5)) * PB_REC;
        u32x4 ml[1]; const unsigned off[1] = {rec + 2 * (lane & 3) * PB_REC + 4096 + (idx & 7) * 16};
        gather<1, 64>(ml, p.ws, off, tag);
        u32x4 v[4];
        const unsigned po = rec + ((lane >> 3) & 3) * 1024 + (idx & 7) * 128 + (lane & 7) * 16;
        asm volatile("global_load_dwordx4 %0, %4, %8 sc1 nt\n\tglobal_load_dwordx4 %1, %5, %8 sc1 nt\n\tglobal_load_dwordx4 %2, %6, %8 sc1 nt\n\tglobal_load_dwordx4 %3, %7, %8 sc1 nt\n\ts_waitcnt vmcnt(0)"
                     : "=&v"(v[0]), "=&v"(v[1]), "=&v"(v[2]), "=&v"(v[3]) : "v"(po), "v"(po + 2 * PB_REC), "v"(po + 4 * PB_REC), "v"(po + 6 * PB_REC), "s"(p.ws) : "memory");
        const float m = dec(ml[0][0]);
        float M = fmaxf(m, dppf<XOR1>(m)); M = fmaxf(M, dppf<XOR2>(M)); M = x32_max(M);
        const float wt = ex2(m - M);
        float L = wt * dec(ml[0][2]); L += dppf<XOR1>(L); L += dppf<XOR2>(L); L = x32_sum(L);
        f32x4 a = asf(v[0]) * dppf<0x00>(wt) + asf(v[1]) * dppf<0x55>(wt) + asf(v[2]) * dppf<0xAA>(wt) + asf(v[3]) * dppf<0xFF>(wt);
#pragma unroll
        for (int r = 0; r < 4; ++r) a[r] = x32_sum(a[r]) / L;
        put2<true>(p.ws, WS_OB + ((((idx >> 3) & 3) * 4 + (idx >> 5)) * 8 + (idx & 7)) * 512 + (lane & 31) * 16, dec(pack2(a[0], a[1])), dec(pack2(a[2], a[3])), tag, 0xffffffffull);
      }
      if (job == 1) {
        nap(w == 3 ? NAP_O - NAP_M : NAP_O);
        u32x4 v[4]; unsigned off[4];
#pragma unroll
        for (int i = 0; i < 4; ++i) off[i] = WS_OB + ((qd >> 4) * 4 + w) * 4096 + i * 1024 + lane * 16;
        gather<4, 64>(v, p.ws, off, tag);
#pragma unroll
        for (int i = 0; i < 4; ++i) *(LAS u32x2*)(L_VB + w * 2112 + i * 512 + lane * 8) = u32x2{v[i][0], v[i][2]};
        lds_signal(C_VB);
        lds_wait(C_VB, 4);
        lds_wait(C_B, 4);
        const f32x4 d = piece4(lane, w);
        if (g < 2) *(LAS f32x2*)(L_OA + w * 256 + (lane & 31) * 8) = g ? f32x2{d[2], d[3]} : f32x2{d[0], d[1]};
        publish256<true>(p.ws, WS_YB + qd * 2048, C_AGG2, lane, tag);
        if (w == 1) {
          nap(2);
          u32x4 y[2];
          const unsigned base = WS_YB + ((lane >> 5) * 16 + (qd & 15)) * 2048 + (qd >> 4) * 512 + (lane & 31) * 16, yb[2] = {base, base + 32 * 2048};
          gather<2, 64>(y, p.ws, yb, tag);
          const float a = x32_sum(dec(y[0][0]) + dec(y[1][0])), b = x32_sum(dec(y[0][2]) + dec(y[1][2]));
          store_bf16(p.out, ((2 * (g & 1) + (g >> 1)) * 2 * DM + DM + 64 * (qd & 15) + 16 * (qd >> 4) + c) * 2, g < 2 ? a : b);
        }
      }
    }
    lds_wait(C_KEND, 4);
    {
      const float mL = *(const LAS float*)(L_PART + w * 8);
      float l = 0.f;
#pragma unroll
      for (int i = 0; i < 2; ++i) {
        const u32x4 sc = lds128(L_SC + w * 2048 + i * 1024 + lane * 16);
#pragma unroll
        for (int k = 0; k < 4; ++k) l += prob(lo16(sc[k]), mL) + prob(hi16(sc[k]), mL);
      }
      l += dppf<XOR1>(l); l += dppf<XOR2>(l); l += dppf<ROR4>(l); l += dppf<ROR8>(l); l = x32_sum(x16_sum(l));
      *(LAS float*)(L_PART + w * 8 + 4) = l;
      lds_signal(C_ML);
    }
    f32x4 fw = {0.f, 0.f, 0.f, 0.f};
    if (w == 0) {
      lds_wait(C_ML, 4);
      const f32x4 m01 = asf(lds128(L_PART)), m23 = asf(lds128(L_PART + 16));
      const float M = fmaxf(fmaxf(m01[0], m01[2]), fmaxf(m23[0], m23[2]));
      const float L = (ex2(m01[0] - M) * m01[1] + ex2(m01[2] - M) * m01[3]) + (ex2(m23[0] - M) * m23[1] + ex2(m23[2] - M) * m23[3]);
      put2(p.ws, WS_ML + ja * 128 + (lane & 7) * 16, M, L, tag, 0xffull);
      nap(3);
      u32x4 o[1]; const unsigned oo[1] = {WS_ML + (ja ^ 1) * 128 + (lane & 7) * 16};
      gather<1, 64>(o, p.ws, oo, tag);
      const float MM = fmaxf(M, dec(o[0][0])), LL = ex2(M - MM) * L + ex2(dec(o[0][0]) - MM) * dec(o[0][2]);
      fw = f32x4{ex2(m01[0] - MM), ex2(m01[2] - MM), ex2(m23[0] - MM), ex2(m23[2] - MM)} / LL;
    }
    pin(fw);
    unsigned off[4], yo[2], pa = WS_PA + (8 * ((ja >> 1) & 31) + 2 * (ja >> 6) + (ja & 1)) * 1024 + lane * 16;
#pragma unroll
    for (int i = 0; i < 4; ++i) { off[i] = WS_PA + ((jo & ~15) + 8 * (i >> 1) + 2 * w + (i & 1)) * 1024 + lane * 16; pin(off[i]); }
    const int n = 64 * (jo & 15) + 16 * w + c;
    unsigned ya = WS_YA + ((g & 1) * 128 + (n >> 3)) * 2048 + (jo >> 4) * 128 + (n & 7) * 16;
    unsigned oa = ((2 * (j >> 7) + ((lane >> 3) & 1)) * 2 * DM + 8 * (j & 127) + (lane & 7)) * 2;
    unsigned lp = L_PART + 64 + 8 * lane, lw = L_OA + w * 576 + 4 * lane, lr = L_OA + (lane & 3) * 576 + 16 * g;
    yo[0] = WS_YA + j * 2048 + lane * 16; yo[1] = yo[0] + 1024;
    pin(pa); pin(ya); pin(oa); pin(lp); pin(lw); pin(lr); pin(yo[0]); pin(yo[1]);
    if (w != 0) {
      u32x4 unused;
      asm volatile("global_load_dwordx4 %0, %1, %2 sc1\n\ts_waitcnt vmcnt(0)" : "=&v"(unused) : "v"(w == 1 ? pa : yo[w & 1]), "s"(p.ws) : "memory");
    }
    wg_barrier();
    if (w == 0) {
      const f32x2 s = (fw[0] * lds64f(lp) + fw[1] * lds64f(lp + 512)) + (fw[2] * lds64f(lp + 1024) + fw[3] * lds64f(lp + 1536));
      put2(p.ws, pa, s[0], s[1], tag, ~0ull);
    }
    u32x4 v[4];
    __builtin_amdgcn_s_sleep(SLEEP_PA);
    gather<4, 1>(v, p.ws, off, tag);
    *(LAS unsigned*)lw = pack2(dec(v[0][0]) + dec(v[1][0]), dec(v[0][2]) + dec(v[1][2]));
    *(LAS unsigned*)(lw + 256) = pack2(dec(v[2][0]) + dec(v[3][0]), dec(v[2][2]) + dec(v[3][2]));
    wg_barrier();
    const f32x4 d = piece(lr, F, f32x4{0.f, 0.f, 0.f, 0.f});
    put2(p.ws, ya, g ? d[2] : d[0], g ? d[3] : d[1], tag, 0xffffffffull);
    if (w == 0) {
      u32x4 y[2];
      __builtin_amdgcn_s_sleep(SLEEP_Y);
      gather<2, 0>(y, p.ws, yo, tag);
      const float a = dec(y[0][0]) + dec(y[1][0]), b = dec(y[0][2]) + dec(y[1][2]);
      const float k = (lane & 8) ? b : a, sd = (lane & 8) ? a : b;
      const float r = x32_sum(x16_sum(k + dppf<ROR8>(sd)));
      if (lane < 16) store_bf16(p.out, oa, r);
      if (j == 0 && lane == 0) __hip_atomic_store((GAS u64*)(p.ws + WS_EPOCH), epoch + 1, __ATOMIC_RELAXED, __HIP_MEMORY_SCOPE_AGENT);
    }
    return;
  }

  u32x4 R[8];
  const unsigned vq = wtile_voff(lane, wave & 1, HD * 2);
  const char* const wq = (const char*)p.wqA + ((size_t)((j & 3) * 256 + wave * 8) * HD + (jq >> 2) * 64) * 2;
  static_for<0, 8>([&](auto ic) __attribute__((always_inline)) {
    constexpr int i = decltype(ic)::value;
    u32x4& r = R[i];
    asm volatile("" : "=v"(r));
    aload<0>(r, vq, wq + (size_t)i * 32 * HD * 2);
  });
  int jb = job; asm volatile("" : "+s"(jb));
  const unsigned vo = wtile_voff(lane, wave & 1, DM * 2);
  const char* const wo = (const char*)p.woA + ((size_t)((jo >> 4) * 256 + wave * 8) * DM + (jo & 15) * 64) * 2;
  const unsigned vb = jb == 0 ? vq : jb == 1 ? vo : g * 256 + 16 * (c ^ (g << 2));
  unsigned bstep = jb == 0 ? 8 * HD * 2 : jb == 1 ? 8 * DM * 2 : 1024;
  asm volatile("" : "+s"(bstep));
  const unsigned boff = (jb == 0 ? qd * 128 : jb == 1 ? ((qd >> 4) * 1024 * DM + (qd & 15) * 64) * 2
                                                      : ((idx >> 3) * SK + (SK - WIN) + (idx & 6) * 256 + (idx & 1) * 128) * DH * 2) + wave * bstep;
  const unsigned bnext = 4 * bstep, bjump = (jb >= 2 ? 36 : 4) * bstep;
  const char* bp = (const char*)(jb == 0 ? p.wqB : jb == 1 ? p.woB : p.kB) + boff;
  const char* const b23 = jb < 2 ? bp + 64 * bstep : (const char*)p.vB + boff;
  const unsigned voffV = lane * 16, voffK = ((lane >> 2) & 3) * 256 + g * 64 + (lane & 3) * 16;
  const unsigned ldst = wave * 1024 + lane * 16;
  const size_t a0 = (size_t)(ja >> 1) * SK * DH * 2 + (ja & 1) * 32768 + wave * 1024 + 4096;
  const char* const kp = (const char*)p.kA + a0;
  const char* const vp = (const char*)p.vA + a0;
  static_for<8, 56>([&](auto nc) __attribute__((always_inline)) {
    constexpr int n = decltype(nc)::value, k = n >> 3, i = n & 7;
    constexpr unsigned reg = ((k - 1) & 3) * 32768;
    if constexpr (n == 40) lds_wait(C_FREE0, 4);
    if constexpr (n == 48) lds_wait(C_FREE1, 4);
    stage<n < 16 ? 7 : 5, (reg & 0xffffu) + i * 4096>(R[i], ldst + (reg & ~0xffffu));
    if constexpr (k == 1) aload<0>(R[i], vo, wo + (size_t)i * 32 * DM * 2);
    if constexpr (k >= 2 && k <= 5) {
      if constexpr (n == 32) bp = b23;
      aload<0>(R[i], vb, bp);
      bp += i == 7 ? bjump : bnext;
    }
    if constexpr (k == 6) sload<i>(R[i], voffK, kp);
    if constexpr (n == 15) wg_barrier();
    if constexpr (n == 23) lds_signal(C_WOA);
    if constexpr (n == 55) lds_signal(C_B);
  });
  lds_wait(C_QA, 1);
  unsigned due = (__builtin_amdgcn_readfirstlane(*(const LAS unsigned*)(L_CNT + 4 * C_T0)) + SCH_START) * 16u;
  int prev = 1 << 20;
  const unsigned sca = L_SC + wave * 2048 + (lane >> 2) * 4;
  float mx = -INFINITY;
  {
    u32x4 QB[4];
    const u32x4 qv = lds128(L_QA + 16 * (4 * g + (lane & 3)));
#pragma unroll
    for (int i = 0; i < 4; ++i) QB[i] = ((c >> 2) == i) ? qv : u32x4{0u, 0u, 0u, 0u};
#pragma unroll 1
    for (unsigned r = 1; r <= 32; ++r) {
      const unsigned sc = score_step(R, QB, r == 32 ? voffV : voffK, r == 32 ? vp : kp + r * 65536, pace_depth(due, prev, SCH_EARLY));
      *(LAS unsigned*)(sca + (r - 1) * 64) = sc;
      mx = fmaxf(mx, fmaxf(lo16(sc), hi16(sc)));
    }
  }
  mx = fmaxf(mx, dppf<ROR4>(mx)); mx = fmaxf(mx, dppf<ROR8>(mx));
  const float mL = mulL(x32_max(x16_max(mx)));
  *(LAS float*)(L_PART + wave * 8) = mL;
  lds_signal(C_KEND);
  unsigned msk[4];
#pragma unroll
  for (int i = 0; i < 4; ++i) msk[i] = (c == 2 * i) ? 0x0000ffffu : (c == 2 * i + 1) ? 0xffff0000u : 0u;
  f32x4 acc = {0.f, 0.f, 0.f, 0.f};
#pragma unroll 1
  for (unsigned r = 1; r < 32; ++r)
    value_step<true>(acc, mL, *(const LAS unsigned*)(sca + (r - 1) * 64), R, msk, voffV, vp + r * 65536, pace_depth(due, prev, r <= SCH_KNEE ? SCH_EARLY : SCH_LATE));
  unsigned pw = L_PART + 64 + wave * 512 + (32 * g + c) * 4; pin(pw);
  value_step<false>(acc, mL, *(const LAS unsigned*)(sca + 31 * 64), R, msk, 0u, nullptr, 0u);
  if (c < 8) {
#pragma unroll
    for (int r = 0; r < 4; ++r) *(LAS float*)(pw + 32 * r) = acc[r];
  }
  wg_barrier();
}

extern "C" void kernel_launch(void* const* d_in, const int* in_sizes, int n_in, void* d_out, int out_size, void* d_ws, size_t ws_size, hipStream_t stream) {
  Params p;
  p.xA = (const u16*)d_in[0]; p.kA = (const u16*)d_in[1]; p.vA = (const u16*)d_in[2]; p.wqA = (const u16*)d_in[3]; p.woA = (const u16*)d_in[4];
  p.xB = (const u16*)d_in[5]; p.kB = (const u16*)d_in[6]; p.vB = (const u16*)d_in[7]; p.wqB = (const u16*)d_in[8]; p.woB = (const u16*)d_in[9];
  p.out = (u16*)d_out; p.ws = (unsigned char*)d_ws;
  (void)hipFuncSetAttribute((const void*)fused_decode, hipFuncAttributeMaxDynamicSharedMemorySize, LDS_BYTES);
  fused_decode<<<NWG, NTHR, LDS_BYTES, stream>>>(p);
}
```

```cpp
#include <hip/hip_runtime.h>
#include <hip/hip_bf16.h>
#include <cstdint>
#pragma clang diagnostic ignored "-Wint-to-pointer-cast"

typedef __attribute__((ext_vector_type(4))) unsigned u32x4;
typedef __attribute__((ext_vector_type(2))) unsigned u32x2;
typedef __attribute__((ext_vector_type(4))) float f32x4;
typedef __attribute__((ext_vector_type(2))) float f32x2;
typedef __attribute__((ext_vector_type(8))) __bf16 bf16x8;
typedef __attribute__((ext_vector_type(2))) __bf16 bf16x2;
typedef __attribute__((ext_vector_type(4))) short s16x4;
typedef unsigned short u16;
typedef unsigned long long u64;
#define LAS __attribute__((address_space(3)))
#define GAS __attribute__((address_space(1)))
#define INL __device__ __forceinline__

constexpr int DM = 1024, DH = 128, SK = 8192, WIN = 2048, HD = 4096;
constexpr float SCALE = 0.088388347648318440f, LOG2E = 1.4426950408889634f;
constexpr int NWG = 256, NTHR = 512;

constexpr int SCH_START = 765, SCH_EARLY = 1747, SCH_LATE = 1711, SCH_END = 1500, SCH_JUMP = 185;
constexpr int NAP_Q = 6, NAP_M = 12, NAP_O = 18, SLEEP_PA = 16, SLEEP_Y = 12;

constexpr unsigned WS_EPOCH = 0;
constexpr unsigned WS_T0 = 4096;
constexpr unsigned WS_QA = WS_T0 + NWG * 16;
constexpr unsigned WS_PA = WS_QA + NWG * 2048;
constexpr unsigned WS_ML = WS_PA + NWG * 1024;
constexpr unsigned WS_YA = WS_ML + NWG * 128;
constexpr unsigned WS_QB = WS_YA + NWG * 2048;
constexpr unsigned PB_REC = 4096 + 128;
constexpr unsigned WS_PB = WS_QB + 64 * 1024;
constexpr unsigned WS_OB = WS_PB + 128 * PB_REC;
constexpr unsigned WS_YB = WS_OB + 128 * 512;
constexpr unsigned WS_END = WS_YB + 64 * 2048;
static_assert(WS_END <= (4u << 20), "workspace");

constexpr unsigned L_R0 = 0, L_R1 = 32768, L_R2 = 65536;
constexpr unsigned L_QA = 131072;
constexpr unsigned L_OA = L_QA + 256;
constexpr unsigned L_PART = L_OA + 4 * 576;
constexpr unsigned L_XA = L_PART + 64 + 4 * 512;
constexpr unsigned L_VB = L_XA + 2048;
constexpr unsigned L_SC = L_VB + 4 * 2112;
constexpr unsigned L_CNT = L_SC + 8192;
constexpr unsigned LDS_BYTES = L_CNT + 64;
enum { C_WOA = 0, C_B, C_FREE0, C_FREE1, C_QA, C_VB, C_PB, C_AGG, C_AGG2, C_T0, C_KEND, C_ML };
constexpr unsigned SPIN_CAP = 1u << 17;

struct Params {
  const u16 *wqA, *xA, *kA, *vA, *woA, *xB, *kB, *vB, *wqB, *woB;
  u16* out;
  unsigned char* ws;
};

template <int V> struct IC { static constexpr int value = V; };
template <int A, int B, typename F> INL void static_for(F&& f) { if constexpr (A < B) { f(IC<A>{}); static_for<A + 1, B>(f); } }
INL u16 f2bf(float f) { return __builtin_bit_cast(u16, (__bf16)f); }
INL float bf16r(float f) { return __uint_as_float(((unsigned)f2bf(f)) << 16); }
INL unsigned pack2(float lo, float hi) { return __builtin_bit_cast(unsigned, __builtin_convertvector(f32x2{lo, hi}, bf16x2)); }
INL float lo16(unsigned u) { return __uint_as_float(u << 16); }
INL float hi16(unsigned u) { return __uint_as_float(u & 0xffff0000u); }
template <int CTRL> INL float dppf(float x) { return __builtin_bit_cast(float, __builtin_amdgcn_mov_dpp(__builtin_bit_cast(int, x), CTRL, 0xf, 0xf, true)); }
template <int CTRL> INL unsigned dppu(unsigned x) { return (unsigned)__builtin_amdgcn_mov_dpp((int)x, CTRL, 0xf, 0xf, true); }
constexpr int XOR1 = 0xB1, XOR2 = 0x4E, HMIR = 0x141, ROR4 = 0x124, ROR8 = 0x128, BCAST = 0x150;
INL float x16_max(float x) { auto s = __builtin_amdgcn_permlane16_swap(__float_as_uint(x), __float_as_uint(x), false, false); return fmaxf(__uint_as_float(s[0]), __uint_as_float(s[1])); }
INL float x32_max(float x) { auto s = __builtin_amdgcn_permlane32_swap(__float_as_uint(x), __float_as_uint(x), false, false); return fmaxf(__uint_as_float(s[0]), __uint_as_float(s[1])); }
INL float x16_sum(float x) { auto s = __builtin_amdgcn_permlane16_swap(__float_as_uint(x), __float_as_uint(x), false, false); return __uint_as_float(s[0]) + __uint_as_float(s[1]); }
INL float x32_sum(float x) { auto s = __builtin_amdgcn_permlane32_swap(__float_as_uint(x), __float_as_uint(x), false, false); return __uint_as_float(s[0]) + __uint_as_float(s[1]); }
INL f32x4 mfma16(u32x4 a, u32x4 b, f32x4 c) { return __builtin_amdgcn_mfma_f32_16x16x32_bf16(__builtin_bit_cast(bf16x8, a), __builtin_bit_cast(bf16x8, b), c, 0, 0, 0); }
INL float prob(float s, float m) { return __builtin_amdgcn_exp2f((s - m) * LOG2E); }
INL u32x4 asu(const f32x4& v) { return __builtin_bit_cast(u32x4, v); }
INL f32x4 asf(const u32x4& v) { return __builtin_bit_cast(f32x4, v); }
INL float dec(unsigned v) { return __uint_as_float(v); }
INL float sel4(const f32x4& d, int g) { return (g & 2) ? ((g & 1) ? d[3] : d[2]) : ((g & 1) ? d[1] : d[0]); }
template <typename T> INL void pin(T& d) { asm volatile("" : "+v"(d)); }
INL unsigned clock10ns() { return (unsigned)__builtin_amdgcn_s_memrealtime(); }

INL u32x4 lds128(unsigned a) { return *(const LAS u32x4*)a; }
INL f32x2 lds64f(unsigned a) { return *(const LAS f32x2*)a; }
INL u32x2 lds_tr(unsigned a) { return __builtin_bit_cast(u32x2, __builtin_amdgcn_ds_read_tr16_b64_v4i16((LAS s16x4*)a)); }
INL void lds_signal(int c) {
  u64 keep;
  asm volatile("s_waitcnt lgkmcnt(0)\n\ts_mov_b64 %0, exec\n\ts_mov_b64 exec, 1\n\tds_add_u32 %1, %2\n\ts_mov_b64 exec, %0" : "=&s"(keep) : "v"(L_CNT + 4 * c), "v"(1u) : "memory");
}
INL unsigned lds_ticket(int c) {
  unsigned old; u64 keep;
  asm volatile("s_waitcnt lgkmcnt(0)\n\ts_mov_b64 %1, exec\n\ts_mov_b64 exec, 1\n\tds_add_rtn_u32 %0, %2, %3\n\ts_mov_b64 exec, %1\n\ts_waitcnt lgkmcnt(0)"
               : "=&v"(old), "=&s"(keep) : "v"(L_CNT + 4 * c), "v"(1u) : "memory");
  return __builtin_amdgcn_readfirstlane(old);
}
INL void lds_wait(int c, unsigned n) {
#pragma unroll 1
  for (unsigned s = 0; s < SPIN_CAP * 8; ++s) {
    if ((unsigned)__builtin_amdgcn_readfirstlane(*(volatile LAS unsigned*)(L_CNT + 4 * c)) >= n) break;
    __builtin_amdgcn_s_sleep(1);
  }
  asm volatile("" ::: "memory");
}
INL void wg_barrier() { asm volatile("s_waitcnt lgkmcnt(0)\n\ts_barrier" ::: "memory"); }
INL void nap(int n) {
#pragma unroll 1
  for (int i = 0; i < n; ++i) __builtin_amdgcn_s_sleep(64);
}

template <int OFF> INL void aload(u32x4& d, unsigned voff, const void* sbase) {
  asm volatile("global_load_dwordx4 %0, %1, %2 offset:%3 nt" : "+v"(d) : "v"(voff), "s"(sbase), "i"(OFF) : "memory");
}
template <int I> INL void sload(u32x4& d, unsigned voff, const char* base) {
  aload<(I & 1) * 4096 - 4096>(d, voff, base + (I >> 1) * 8192);
}
template <int N> INL void waitv(u32x4& d) { asm volatile("s_waitcnt vmcnt(%1)" : "+v"(d) : "i"(N) : "memory"); }
template <int N> INL void waitvm() { asm volatile("s_waitcnt vmcnt(%0)" :: "i"(N) : "memory"); }
INL void pacev(u32x4& d, unsigned depth) {
  asm volatile("s_cmp_eq_u32 %1, 7\n\ts_cbranch_scc1 7f\n\ts_cmp_eq_u32 %1, 6\n\ts_cbranch_scc1 6f\n\ts_cmp_eq_u32 %1, 5\n\ts_cbranch_scc1 5f\n\t"
               "s_waitcnt vmcnt(4)\n5:\n\ts_waitcnt vmcnt(5)\n6:\n\ts_waitcnt vmcnt(6)\n7:\n\ts_waitcnt vmcnt(7)" : "+v"(d) : "s"(depth) : "memory", "scc");
}
template <int W, int OFF> INL void stage(u32x4& d, unsigned lds_addr) {
  asm volatile("s_waitcnt vmcnt(%2)\n\tds_write_b128 %1, %0 offset:%3" : "+v"(d) : "v"(lds_addr), "i"(W), "i"(OFF) : "memory");
}
INL unsigned pace_depth(unsigned& due, int& prev, unsigned step) {
  due += step;
  int late = (int)(clock10ns() * 16u - due) >> 4;
  if (late - prev > SCH_JUMP) { due += (unsigned)(late - prev) * 16u; late = prev; }
  prev = late;
  return __builtin_amdgcn_readfirstlane(min(max((late >> 6) + 5, 4), 7));
}

template <bool NT = false> INL void put2(const unsigned char* ws, unsigned off, float a, float b, unsigned tag, u64 mask) {
  u64 keep;
  const u32x4 v = {__float_as_uint(a), tag, __float_as_uint(b), tag};
  if (NT) asm volatile("s_mov_b64 %0, exec\n\ts_mov_b64 exec, %4\n\tglobal_store_dwordx4 %1, %2, %3 sc1 nt\n\ts_mov_b64 exec, %0\n\ts_nop 0"
                       : "=&s"(keep) : "v"(off), "v"(v), "s"(ws), "s"(mask) : "memory");
  else asm volatile("s_mov_b64 %0, exec\n\ts_mov_b64 exec, %4\n\tglobal_store_dwordx4 %1, %2, %3 sc1\n\ts_mov_b64 exec, %0\n\ts_nop 0"
                    : "=&s"(keep) : "v"(off), "v"(v), "s"(ws), "s"(mask) : "memory");
}
INL void poll16(u32x4& d, const unsigned char* ws, unsigned off, unsigned need) {
  asm volatile("s_cmp_eq_u32 %3, 0\n\ts_cbranch_scc1 1f\n\tglobal_load_dwordx4 %0, %1, %2 sc1\n1:" : "+v"(d) : "v"(off), "s"(ws), "s"(need) : "memory", "scc");
}
template <int N, int SLEEP> INL void gather(u32x4 (&v)[N], const unsigned char* ws, const unsigned (&off)[N], unsigned tag) {
  unsigned need[N];
#pragma unroll
  for (int i = 0; i < N; ++i) { asm volatile("" : "=v"(v[i])); need[i] = 1; }
#pragma unroll 1
  for (unsigned s = 0; s < SPIN_CAP; ++s) {
#pragma unroll
    for (int i = 0; i < N; ++i) poll16(v[i], ws, off[i], need[i]);
    waitvm<0>();
    unsigned bad[N], any = 0;
#pragma unroll
    for (int i = 0; i < N; ++i) pin(v[i]);
#pragma unroll
    for (int i = 0; i < N; ++i) { bad[i] = (v[i][1] ^ tag) | (v[i][3] ^ tag); any |= bad[i]; }
    pin(any);
    if (__builtin_amdgcn_ballot_w64(any != 0) == 0) break;
#pragma unroll
    for (int i = 0; i < N; ++i) { const u64 m = __builtin_amdgcn_ballot_w64(bad[i] != 0); need[i] = (unsigned)m | (unsigned)(m >> 32); }
    if (SLEEP) __builtin_amdgcn_s_sleep(SLEEP);
  }
}
template <bool NT> INL void publish256(const unsigned char* ws, unsigned off, int cnt, int lane, unsigned tag) {
  if (lds_ticket(cnt) == 3) {
    const f32x2 e0 = lds64f(L_OA + lane * 8), e1 = lds64f(L_OA + 512 + lane * 8);
    put2<NT>(ws, off + lane * 16, e0[0], e0[1], tag, ~0ull);
    put2<NT>(ws, off + 1024 + lane * 16, e1[0], e1[1], tag, ~0ull);
  }
}
INL void store_bf16(u16* out, unsigned byte_off, float v) { asm volatile("global_store_short %0, %1, %2 sc1" :: "v"(byte_off), "v"(pack2(v, v)), "s"(out) : "memory"); }

INL unsigned score_step(u32x4 (&R)[8], const u32x4 (&QB)[4], unsigned voff, const char* next, unsigned depth) {
  f32x4 s[2] = {{0.f, 0.f, 0.f, 0.f}, {0.f, 0.f, 0.f, 0.f}};
  static_for<0, 8>([&](auto ic) __attribute__((always_inline)) {
    constexpr int i = decltype(ic)::value;
    pacev(R[i], depth);
    s[i >> 2] = mfma16(R[i], QB[i & 3], s[i >> 2]);
    __builtin_amdgcn_sched_barrier(0);
    sload<i>(R[i], voff, next);
    __builtin_amdgcn_sched_barrier(0);
  });
  float d = dppf<0x00>(s[0][0]); d += dppf<0x55>(s[0][1]); d += dppf<0xAA>(s[0][2]); d += dppf<0xFF>(s[0][3]);
  float e = dppf<0x00>(s[1][0]); e += dppf<0x55>(s[1][1]); e += dppf<0xAA>(s[1][2]); e += dppf<0xFF>(s[1][3]);
  const unsigned u = pack2(d, e);
  return pack2(lo16(u) * SCALE, hi16(u) * SCALE);
}
template <bool MORE>
INL void value_step(f32x4& acc, float m, unsigned sc, u32x4 (&R)[8], const unsigned (&msk)[4], unsigned voff, const char* next, unsigned depth) {
  const float pa = prob(lo16(sc), m), pb = prob(hi16(sc), m);
  unsigned pk[2] = {pack2(pa, pa), pack2(pb, pb)};
  pin(pk[0]); pin(pk[1]);
  static_for<0, 8>([&](auto ic) __attribute__((always_inline)) {
    constexpr int i = decltype(ic)::value;
    const unsigned b = pk[i >> 2];
    u32x4 P = {dppu<BCAST + 4 * (i & 3)>(b) & msk[0], dppu<BCAST + 4 * (i & 3)>(b) & msk[1], dppu<BCAST + 4 * (i & 3)>(b) & msk[2], dppu<BCAST + 4 * (i & 3)>(b) & msk[3]};
    if constexpr (MORE) pacev(R[i], depth); else { pin(P); waitv<7 - i>(R[i]); }
    acc = mfma16(R[i], P, acc);
    __builtin_amdgcn_sched_barrier(0);
    if constexpr (MORE) sload<i>(R[i], voff, next);
  });
}

INL void load_wfrag(u32x4 (&B)[8], unsigned region, int w, int lane) {
  const int g = lane >> 4, q = (lane >> 2) & 3, pp = lane & 3;
  const unsigned sw = 2u * ((q >> 1) | ((g & 1) << 1));
  const unsigned a = region + (8 * g + q) * 128 + 16 * ((2 * w + (pp >> 1)) ^ sw) + 8 * (pp & 1);
#pragma unroll
  for (int kk = 0; kk < 8; ++kk) { const u32x2 lo = lds_tr(a + kk * 4096), hi = lds_tr(a + kk * 4096 + 512); B[kk] = u32x4{lo[0], lo[1], hi[0], hi[1]}; }
}
INL f32x4 piece(unsigned a, const u32x4 (&B)[8], f32x4 d) {
  u32x4 A[8];
#pragma unroll
  for (int kk = 0; kk < 8; ++kk) A[kk] = lds128(a + 64 * kk);
  __builtin_amdgcn_sched_barrier(0);
#pragma unroll
  for (int kk = 0; kk < 8; ++kk) d = mfma16(A[kk], B[kk], d);
  return d;
}
INL f32x4 piece4(int lane, int w) {
  f32x4 d = {0.f, 0.f, 0.f, 0.f};
#pragma unroll 1
  for (int tt = 0; tt < 4; ++tt) { u32x4 B[8]; load_wfrag(B, ((tt + 2) & 3) * 32768, w, lane); d = piece(L_VB + tt * 512 + (lane & 3) * 2112 + 16 * (lane >> 4), B, d); }
  return d;
}
INL unsigned wtile_voff(int lane, int odd, unsigned rs) {
  const unsigned sw = 2u * (((lane >> 4) & 1) | (odd << 1));
  return (lane >> 3) * rs + 16 * ((lane & 7) ^ sw);
}

__global__ void __launch_bounds__(NTHR, 2) fused_decode(Params p) {
  const int lane = threadIdx.x & 63, wave = __builtin_amdgcn_readfirstlane(threadIdx.x >> 6);
  const int j = blockIdx.x, g = lane >> 4, c = lane & 15;
  const int ja = (j & ~15) | ((j & 7) << 1) | ((j >> 3) & 1), jq = j ^ 8, jo = ((j & 15) << 4) | (j >> 4);
  const int job = (j >> 3) & 3, qd = ((j & 7) << 3) | (j >> 5), idx = job >= 2 ? ((j & 7) << 4) | ((job & 1) << 3) | (j >> 5) : 2 * qd + job;

  if (wave >= 4) {
    const int w = wave - 4;
    const unsigned th = clock10ns();
    u32x4 xv = {0u, 0u, 0u, 0u};
    if (w < 2) asm volatile("global_load_dwordx4 %0, %1, %2" : "+v"(xv) : "v"((lane >> 5) * DM * 2 + (lane & 31) * 16), "s"((const char*)p.xA + (w * 2 * DM + (j & 3) * 256) * 2) : "memory");
    if (w == 3 && lane < 16) *(LAS unsigned*)(L_CNT + 4 * lane) = 0u;
    u32x2 ev;
    asm volatile("global_load_dwordx2 %0, %1, %2 sc1\n\ts_waitcnt vmcnt(0)" : "=&v"(ev) : "v"(WS_EPOCH), "s"(p.ws) : "memory");
    const u64 epoch = ((u64)(unsigned)__builtin_amdgcn_readfirstlane(ev[1]) << 32) | (unsigned)__builtin_amdgcn_readfirstlane(ev[0]);
    u64 z = epoch + 0x9E3779B97F4A7C15ull;
    z = (z ^ (z >> 30)) * 0xBF58476D1CE4E5B9ull; z = (z ^ (z >> 27)) * 0x94D049BB133111EBull; z ^= z >> 31;
    const unsigned tag = (unsigned)(z >> 32);
    if (w == 2) put2(p.ws, WS_T0 + jq * 16, dec(th), dec(th), tag, 1ull);
    if (w < 2) { waitv<0>(xv); *(LAS u32x4*)(L_XA + w * 1024 + lane * 16) = xv; }
    wg_barrier();
    u32x4 F[8];
    load_wfrag(F, L_R0, w, lane);
    *(LAS float*)(L_OA + (g * 64 + 16 * w + c) * 4) = sel4(piece(L_XA + (lane & 3) * 512 + 16 * g, F, f32x4{0.f, 0.f, 0.f, 0.f}), g);
    publish256<false>(p.ws, WS_QA + jq * 2048, C_AGG, lane, tag);
    lds_signal(C_FREE0);
    lds_wait(C_WOA, 4);
    load_wfrag(F, L_R1, w, lane);
#pragma unroll
    for (int kk = 0; kk < 8; ++kk) pin(F[kk]);
    lds_signal(C_FREE1);
    if (w == 0) {
      u32x4 v[5]; unsigned off[5];
#pragma unroll
      for (int i = 0; i < 4; ++i) off[i] = WS_QA + (8 * ((ja >> 1) & 31) + 4 * (lane >> 5) + i) * 2048 + (ja >> 6) * 512 + (lane & 31) * 16;
      off[4] = WS_T0 + (8 * ((ja >> 1) & 31) + (lane & 7)) * 16;
      gather<5, 8>(v, p.ws, off, tag);
      unsigned dt = v[4][0] - th;
      dt += dppu<XOR1>(dt); dt += dppu<XOR2>(dt); dt += dppu<HMIR>(dt);
      *(LAS unsigned*)(L_CNT + 4 * C_T0) = th + (unsigned)((int)dt >> 3);
      *(LAS unsigned*)(L_QA + 4 * lane) = pack2((dec(v[0][0]) + dec(v[1][0])) + (dec(v[2][0]) + dec(v[3][0])), (dec(v[0][2]) + dec(v[1][2])) + (dec(v[2][2]) + dec(v[3][2])));
      lds_signal(C_QA);
    }
    if (job >= 2) {
      {
        nap(NAP_Q);
        u32x4 v[1]; const unsigned off[1] = {WS_QB + (((idx >> 3) & 3) * 16 + 4 * w + g) * 1024 + (idx >> 5) * 256 + c * 16};
        gather<1, 64>(v, p.ws, off, tag);
        *(LAS u32x2*)(L_VB + (2 * w + (lane >> 5)) * 272 + (lane & 31) * 8) = u32x2{v[0][0], v[0][2]};
        lds_signal(C_VB);
      }
      lds_wait(C_VB, 4);
      lds_wait(C_B, 4);
      u32x4 Q[4];
#pragma unroll
      for (int kk = 0; kk < 4; ++kk) Q[kk] = lds128(L_VB + (c & 7) * 272 + 64 * kk + 16 * g);
      float s[16][4]; float mx = -INFINITY;
#pragma unroll
      for (int blk = 0; blk < 16; ++blk) {
        f32x4 d = {0.f, 0.f, 0.f, 0.f};
#pragma unroll
        for (int kk = 0; kk < 4; ++kk) d = mfma16(lds128(L_R2 + (16 * blk + c) * 256 + 16 * ((4 * kk + g) ^ ((c & 3) << 2))), Q[kk], d);
#pragma unroll
        for (int r = 0; r < 4; ++r) { s[blk][r] = bf16r(bf16r(d[r]) * SCALE); mx = fmaxf(mx, s[blk][r]); }
      }
      mx = x32_max(x16_max(mx));
      float l = 0.f;
#pragma unroll
      for (int blk = 0; blk < 16; ++blk)
#pragma unroll
        for (int r = 0; r < 4; ++r) { s[blk][r] = prob(s[blk][r], mx); l += s[blk][r]; }
      l = x32_sum(x16_sum(l));
      f32x4 o0 = {0.f, 0.f, 0.f, 0.f}, o1 = {0.f, 0.f, 0.f, 0.f};
      const int q = (lane >> 2) & 3, pp = lane & 3;
      const unsigned va0 = L_R0 + (4 * g + q) * 256 + 16 * ((4 * w + (pp >> 1)) ^ (q << 2)) + 8 * (pp & 1);
#pragma unroll
      for (int tt = 0; tt < 8; ++tt) {
        const u32x4 P = {pack2(s[2 * tt][0], s[2 * tt][1]), pack2(s[2 * tt][2], s[2 * tt][3]), pack2(s[2 * tt + 1][0], s[2 * tt + 1][1]), pack2(s[2 * tt + 1][2], s[2 * tt + 1][3])};
        const u32x2 a0 = lds_tr(va0 + tt * 8192), a1 = lds_tr(va0 + tt * 8192 + 4096), b0 = lds_tr(va0 + tt * 8192 + 32), b1 = lds_tr(va0 + tt * 8192 + 4096 + 32);
        o0 = mfma16(u32x4{a0[0], a0[1], a1[0], a1[1]}, P, o0);
        o1 = mfma16(u32x4{b0[0], b0[1], b1[0], b1[1]}, P, o1);
      }
      asm volatile("global_store_dwordx4 %0, %1, %2 sc1 nt\n\ts_waitcnt vmcnt(0)"
                   :: "v"(WS_PB + idx * PB_REC + w * 1024 + (c & 7) * 128 + (c >> 3) * 64 + g * 16), "v"(asu(c < 8 ? o0 : o1)), "s"(p.ws) : "memory");
      if (lds_ticket(C_PB) == 3) put2<true>(p.ws, WS_PB + idx * PB_REC + 4096 + (lane & 7) * 16, mx, l, tag, 0xffull);
    } else {
      if (job == 0) {
        u32x4 v0, v1;
        asm volatile("global_load_dwordx4 %0, %2, %3\n\tglobal_load_dwordx4 %1, %2, %3 offset:1024\n\ts_waitcnt vmcnt(0)" : "=&v"(v0), "=&v"(v1) : "v"(lane * 16), "s"(p.xB + w * DM) : "memory");
        *(LAS u32x4*)(L_VB + w * 2112 + lane * 16) = v0; *(LAS u32x4*)(L_VB + w * 2112 + 1024 + lane * 16) = v1;
        lds_signal(C_VB);
        lds_wait(C_VB, 4);
        lds_wait(C_B, 4);
        *(LAS u16*)(L_OA + (g * 64 + 16 * w + c) * 2) = f2bf(sel4(piece4(lane, w), g));
        if (lds_ticket(C_AGG2) == 3) { const f32x2 e = lds64f(L_OA + lane * 8); put2<true>(p.ws, WS_QB + qd * 1024 + lane * 16, e[0], e[1], tag, ~0ull); }
      }
      if (w == 3) {
        nap(NAP_M);
        const unsigned rec = WS_PB + ((idx & ~7) + (lane >> 5)) * PB_REC;
        u32x4 ml[1]; const unsigned off[1] = {rec + 2 * (lane & 3) * PB_REC + 4096 + (idx & 7) * 16};
        gather<1, 64>(ml, p.ws, off, tag);
        __builtin_amdgcn_fence(__ATOMIC_ACQUIRE, "agent");
        u32x4 v[4];
        const unsigned po = rec + ((lane >> 3) & 3) * 1024 + (idx & 7) * 128 + (lane & 7) * 16;
        asm volatile("global_load_dwordx4 %0, %4, %8 sc1 nt\n\tglobal_load_dwordx4 %1, %5, %8 sc1 nt\n\tglobal_load_dwordx4 %2, %6, %8 sc1 nt\n\tglobal_load_dwordx4 %3, %7, %8 sc1 nt\n\ts_waitcnt vmcnt(0)"
                     : "=&v"(v[0]), "=&v"(v[1]), "=&v"(v[2]), "=&v"(v[3]) : "v"(po), "v"(po + 2 * PB_REC), "v"(po + 4 * PB_REC), "v"(po + 6 * PB_REC), "s"(p.ws) : "memory");
        const float m = dec(ml[0][0]);
        float M = fmaxf(m, dppf<XOR1>(m)); M = fmaxf(M, dppf<XOR2>(M)); M = x32_max(M);
        const float wt = prob(m, M);
        float L = wt * dec(ml[0][2]); L += dppf<XOR1>(L); L += dppf<XOR2>(L); L = x32_sum(L);
        f32x4 a = asf(v[0]) * dppf<0x00>(wt) + asf(v[1]) * dppf<0x55>(wt) + asf(v[2]) * dppf<0xAA>(wt) + asf(v[3]) * dppf<0xFF>(wt);
#pragma unroll
        for (int r = 0; r < 4; ++r) a[r] = x32_sum(a[r]) / L;
        put2<true>(p.ws, WS_OB + ((((idx >> 3) & 3) * 4 + (idx >> 5)) * 8 + (idx & 7)) * 512 + (lane & 31) * 16, dec(pack2(a[0], a[1])), dec(pack2(a[2], a[3])), tag, 0xffffffffull);
      }
      if (job == 1) {
        nap(w == 3 ? NAP_O - NAP_M : NAP_O);
        u32x4 v[4]; unsigned off[4];
#pragma unroll
        for (int i = 0; i < 4; ++i) off[i] = WS_OB + ((qd >> 4) * 4 + w) * 4096 + i * 1024 + lane * 16;
        gather<4, 64>(v, p.ws, off, tag);
#pragma unroll
        for (int i = 0; i < 4; ++i) *(LAS u32x2*)(L_VB + w * 2112 + i * 512 + lane * 8) = u32x2{v[i][0], v[i][2]};
        lds_signal(C_VB);
        lds_wait(C_VB, 4);
        lds_wait(C_B, 4);
        const f32x4 d = piece4(lane, w);
        if (g < 2) *(LAS f32x2*)(L_OA + w * 256 + (lane & 31) * 8) = g ? f32x2{d[2], d[3]} : f32x2{d[0], d[1]};
        publish256<true>(p.ws, WS_YB + qd * 2048, C_AGG2, lane, tag);
        if (w == 1) {
          nap(2);
          u32x4 y[2];
          const unsigned base = WS_YB + ((lane >> 5) * 16 + (qd & 15)) * 2048 + (qd >> 4) * 512 + (lane & 31) * 16, yb[2] = {base, base + 32 * 2048};
          gather<2, 64>(y, p.ws, yb, tag);
          const float a = x32_sum(dec(y[0][0]) + dec(y[1][0])), b = x32_sum(dec(y[0][2]) + dec(y[1][2]));
          store_bf16(p.out, ((2 * (g & 1) + (g >> 1)) * 2 * DM + DM + 64 * (qd & 15) + 16 * (qd >> 4) + c) * 2, g < 2 ? a : b);
        }
      }
    }
    lds_wait(C_KEND, 4);
    {
      const float m = *(const LAS float*)(L_PART + w * 8);
      float l = 0.f;
#pragma unroll
      for (int i = 0; i < 2; ++i) {
        const u32x4 sc = lds128(L_SC + w * 2048 + i * 1024 + lane * 16);
#pragma unroll
        for (int k = 0; k < 4; ++k) l += prob(lo16(sc[k]), m) + prob(hi16(sc[k]), m);
      }
      l += dppf<XOR1>(l); l += dppf<XOR2>(l); l += dppf<ROR4>(l); l += dppf<ROR8>(l); l = x32_sum(x16_sum(l));
      *(LAS float*)(L_PART + w * 8 + 4) = l;
      lds_signal(C_ML);
    }
    f32x4 fw = {0.f, 0.f, 0.f, 0.f};
    if (w == 0) {
      lds_wait(C_ML, 4);
      const f32x4 m01 = asf(lds128(L_PART)), m23 = asf(lds128(L_PART + 16));
      const float M = fmaxf(fmaxf(m01[0], m01[2]), fmaxf(m23[0], m23[2]));
      const float L = (prob(m01[0], M) * m01[1] + prob(m01[2], M) * m01[3]) + (prob(m23[0], M) * m23[1] + prob(m23[2], M) * m23[3]);
      put2(p.ws, WS_ML + ja * 128 + (lane & 7) * 16, M, L, tag, 0xffull);
      nap(3);
      u32x4 o[1]; const unsigned oo[1] = {WS_ML + (ja ^ 1) * 128 + (lane & 7) * 16};
      gather<1, 64>(o, p.ws, oo, tag);
      const float MM = fmaxf(M, dec(o[0][0])), LL = prob(M, MM) * L + prob(dec(o[0][0]), MM) * dec(o[0][2]);
      fw = f32x4{prob(m01[0], MM), prob(m01[2], MM), prob(m23[0], MM), prob(m23[2], MM)} / LL;
    }
    pin(fw);
    unsigned off[4], yo[2], pa = WS_PA + (8 * ((ja >> 1) & 31) + 2 * (ja >> 6) + (ja & 1)) * 1024 + lane * 16;
#pragma unroll
    for (int i = 0; i < 4; ++i) { off[i] = WS_PA + ((jo & ~15) + 8 * (i >> 1) + 2 * w + (i & 1)) * 1024 + lane * 16; pin(off[i]); }
    const int n = 64 * (jo & 15) + 16 * w + c;
    unsigned ya = WS_YA + ((g & 1) * 128 + (n >> 3)) * 2048 + (jo >> 4) * 128 + (n & 7) * 16;
    unsigned oa = ((2 * (j >> 7) + ((lane >> 3) & 1)) * 2 * DM + 8 * (j & 127) + (lane & 7)) * 2;
    unsigned lp = L_PART + 64 + 8 * lane, lw = L_OA + w * 576 + 4 * lane, lr = L_OA + (lane & 3) * 576 + 16 * g;
    yo[0] = WS_YA + j * 2048 + lane * 16; yo[1] = yo[0] + 1024;
    pin(pa); pin(ya); pin(oa); pin(lp); pin(lw); pin(lr); pin(yo[0]); pin(yo[1]);
    if (w == 1) {
      u32x4 unused;
      asm volatile("global_load_dwordx4 %0, %1, %2 sc1\n\ts_waitcnt vmcnt(0)" : "=&v"(unused) : "v"(pa), "s"(p.ws) : "memory");
    }
    wg_barrier();
    if (w == 0) {
      const f32x2 s = (fw[0] * lds64f(lp) + fw[1] * lds64f(lp + 512)) + (fw[2] * lds64f(lp + 1024) + fw[3] * lds64f(lp + 1536));
      put2(p.ws, pa, s[0], s[1], tag, ~0ull);
    }
    u32x4 v[4];
    __builtin_amdgcn_s_sleep(SLEEP_PA);
    gather<4, 1>(v, p.ws, off, tag);
    *(LAS unsigned*)lw = pack2(dec(v[0][0]) + dec(v[1][0]), dec(v[0][2]) + dec(v[1][2]));
    *(LAS unsigned*)(lw + 256) = pack2(dec(v[2][0]) + dec(v[3][0]), dec(v[2][2]) + dec(v[3][2]));
    wg_barrier();
    const f32x4 d = piece(lr, F, f32x4{0.f, 0.f, 0.f, 0.f});
    put2(p.ws, ya, g ? d[2] : d[0], g ? d[3] : d[1], tag, 0xffffffffull);
    if (w == 0) {
      u32x4 y[2];
      __builtin_amdgcn_s_sleep(SLEEP_Y);
      gather<2, 0>(y, p.ws, yo, tag);
      const float a = dec(y[0][0]) + dec(y[1][0]), b = dec(y[0][2]) + dec(y[1][2]);
      const float k = (lane & 8) ? b : a, sd = (lane & 8) ? a : b;
      const float r = x32_sum(x16_sum(k + dppf<ROR8>(sd)));
      if (lane < 16) store_bf16(p.out, oa, r);
      if (j == 0 && lane == 0) __hip_atomic_store((GAS u64*)(p.ws + WS_EPOCH), epoch + 1, __ATOMIC_RELAXED, __HIP_MEMORY_SCOPE_AGENT);
    }
    return;
  }

  u32x4 R[8];
  const unsigned vq = wtile_voff(lane, wave & 1, HD * 2);
  const char* const wq = (const char*)p.wqA + ((size_t)((j & 3) * 256 + wave * 8) * HD + (jq >> 2) * 64) * 2;
  static_for<0, 8>([&](auto ic) __attribute__((always_inline)) {
    constexpr int i = decltype(ic)::value;
    u32x4& r = R[i];
    asm volatile("" : "=v"(r));
    aload<0>(r, vq, wq + (size_t)i * 32 * HD * 2);
  });
  int jb = job; asm volatile("" : "+s"(jb));
  const unsigned vo = wtile_voff(lane, wave & 1, DM * 2);
  const char* const wo = (const char*)p.woA + ((size_t)((jo >> 4) * 256 + wave * 8) * DM + (jo & 15) * 64) * 2;
  const unsigned vb = jb == 0 ? vq : jb == 1 ? vo : g * 256 + 16 * (c ^ (g << 2));
  unsigned bstep = jb == 0 ? 8 * HD * 2 : jb == 1 ? 8 * DM * 2 : 1024;
  asm volatile("" : "+s"(bstep));
  const unsigned boff = (jb == 0 ? qd * 128 : jb == 1 ? ((qd >> 4) * 1024 * DM + (qd & 15) * 64) * 2
                                                      : ((idx >> 3) * SK + (SK - WIN) + (idx & 6) * 256 + (idx & 1) * 128) * DH * 2) + wave * bstep;
  const unsigned bnext = 4 * bstep, bjump = (jb >= 2 ? 36 : 4) * bstep;
  const char* bp = (const char*)(jb == 0 ? p.wqB : jb == 1 ? p.woB : p.kB) + boff;
  const char* const b23 = jb < 2 ? bp + 64 * bstep : (const char*)p.vB + boff;
  const unsigned voffV = lane * 16, voffK = ((lane >> 2) & 3) * 256 + g * 64 + (lane & 3) * 16;
  const unsigned ldst = wave * 1024 + lane * 16;
  const size_t a0 = (size_t)(ja >> 1) * SK * DH * 2 + (ja & 1) * 32768 + wave * 1024 + 4096;
  const char* const kp = (const char*)p.kA + a0;
  const char* const vp = (const char*)p.vA + a0;
  static_for<0, 8>([&](auto ic) __attribute__((always_inline)) {
    constexpr int i = decltype(ic)::value;
    stage<7, i * 4096>(R[i], ldst);
    aload<0>(R[i], vo, wo + (size_t)i * 32 * DM * 2);
  });
  wg_barrier();
  {
    unsigned vn = vb, sn = bnext;
#pragma unroll 1
    for (int k = 1; k <= 5; ++k) {
      if (k == 3) bp = b23;
      if (k == 4) lds_wait(C_FREE0, 4);
      if (k == 5) { lds_wait(C_FREE1, 4); bp = kp - 4096; vn = voffK; sn = 4096; }
      const unsigned dst = ldst + (k & 3) * 32768;
      static_for<0, 8>([&](auto ic) __attribute__((always_inline)) {
        constexpr int i = decltype(ic)::value;
        stage<5, i * 4096>(R[i], dst);
        aload<0>(R[i], vn, bp);
        bp += i == 7 ? bjump : sn;
      });
      if (k == 1) lds_signal(C_WOA);
    }
    lds_signal(C_B);
  }
  lds_wait(C_QA, 1);
  unsigned due = (__builtin_amdgcn_readfirstlane(*(const LAS unsigned*)(L_CNT + 4 * C_T0)) + SCH_START) * 16u;
  int prev = 1 << 20;
  const unsigned sca = L_SC + wave * 2048 + (lane >> 2) * 4;
  float mx = -INFINITY;
  {
    u32x4 QB[4];
    const u32x4 qv = lds128(L_QA + 16 * (4 * g + (lane & 3)));
#pragma unroll
    for (int i = 0; i < 4; ++i) QB[i] = ((c >> 2) == i) ? qv : u32x4{0u, 0u, 0u, 0u};
#pragma unroll 1
    for (unsigned r = 1; r <= 32; ++r) {
      const unsigned sc = score_step(R, QB, r == 32 ? voffV : voffK, r == 32 ? vp : kp + r * 65536, pace_depth(due, prev, SCH_EARLY));
      *(LAS unsigned*)(sca + (r - 1) * 64) = sc;
      mx = fmaxf(mx, fmaxf(lo16(sc), hi16(sc)));
    }
  }
  mx = fmaxf(mx, dppf<ROR4>(mx)); mx = fmaxf(mx, dppf<ROR8>(mx));
  mx = x32_max(x16_max(mx));
  *(LAS float*)(L_PART + wave * 8) = mx;
  lds_signal(C_KEND);
  unsigned msk[4];
#pragma unroll
  for (int i = 0; i < 4; ++i) msk[i] = (c == 2 * i) ? 0x0000ffffu : (c == 2 * i + 1) ? 0xffff0000u : 0u;
  f32x4 acc = {0.f, 0.f, 0.f, 0.f};
#pragma unroll 1
  for (unsigned r = 1; r < 32; ++r)
    value_step<true>(acc, mx, *(const LAS unsigned*)(sca + (r - 1) * 64), R, msk, voffV, vp + r * 65536, pace_depth(due, prev, r <= 8 ? SCH_EARLY : r < 26 ? SCH_LATE : SCH_END));
  unsigned pw = L_PART + 64 + wave * 512 + (32 * g + c) * 4; pin(pw);
  value_step<false>(acc, mx, *(const LAS unsigned*)(sca + 31 * 64), R, msk, 0u, nullptr, 0u);
  if (c < 8) {
#pragma unroll
    for (int r = 0; r < 4; ++r) *(LAS float*)(pw + 32 * r) = acc[r];
  }
  wg_barrier();
}

extern "C" void kernel_launch(void* const* d_in, const int* in_sizes, int n_in, void* d_out, int out_size, void* d_ws, size_t ws_size, hipStream_t stream) {
  Params p;
  p.xA = (const u16*)d_in[0]; p.kA = (const u16*)d_in[1]; p.vA = (const u16*)d_in[2]; p.wqA = (const u16*)d_in[3]; p.woA = (const u16*)d_in[4];
  p.xB = (const u16*)d_in[5]; p.kB = (const u16*)d_in[6]; p.vB = (const u16*)d_in[7]; p.wqB = (const u16*)d_in[8]; p.woB = (const u16*)d_in[9];
  p.out = (u16*)d_out; p.ws = (unsigned char*)d_ws;
  (void)hipFuncSetAttribute((const void*)fused_decode, hipFuncAttributeMaxDynamicSharedMemorySize, LDS_BYTES);
  fused_decode<<<NWG, NTHR, LDS_BYTES, stream>>>(p);
}
```
